```python
import torch

def _softmax(x, axis=-1):
    return torch.softmax(x, dim=axis)

def _gelu(x):
    return torch.nn.functional.gelu(x, approximate='tanh')

def _silu(x):
    return torch.nn.functional.silu(x)

def _sigmoid(x):
    return torch.sigmoid(x)

def _layernorm(x, axis=-1, eps=1e-05):
    m = x.mean(dim=axis, keepdim=True)
    v = x.var(dim=axis, keepdim=True, unbiased=False)
    return (x - m) / torch.sqrt(v + eps)

def _scatter_add(tgt, idx, src, axis=0):
    return tgt.clone().index_add_(axis, idx, src)

def _segment_sum(src, seg, n):
    out = torch.zeros((n,) + src.shape[1:], dtype=src.dtype)
    return out.index_add_(0, seg, src)

def _pad(x, pw):
    flat = tuple((v for pair in reversed(pw) for v in pair))
    return torch.nn.functional.pad(x, flat)
kernel_name = 'recurrent_scan__memory_aug_recurrent_mem_tr__dense_activation_gelu__prompt'

def setup_inputs():

    _w = lambda a: (lambda t: t.to(torch.bfloat16) if t.is_floating_point() else t)(torch.as_tensor(a))

    def _A_setup():
        B, S, Dm = (1, 2048, 640)
        H, Dh = (32, 192)
        M, seg = (8, 32)
        seg = min(seg, S)
        S = S // seg * seg
        return {'x': _w((lambda *s: torch.randn(*s, dtype=torch.bfloat16) * 0.1)(B, S, Dm)), 'mem0': _w((lambda *s: torch.randn(*s, dtype=torch.bfloat16) * 1.6)(B, M, Dm)), 'Wq': _w((lambda *s: torch.randn(*s, dtype=torch.bfloat16) * 0.8)(Dm, H, Dh) * Dm ** (-0.5)), 'Wk': _w((lambda *s: torch.randn(*s, dtype=torch.bfloat16) * 0.8)(Dm, H, Dh) * Dm ** (-0.5)), 'Wv': _w((lambda *s: torch.randn(*s, dtype=torch.bfloat16) * 1.6)(Dm, H, Dh) * Dm ** (-0.5)), 'Wo': _w((lambda *s: torch.randn(*s, dtype=torch.bfloat16) * 1.6)(H, Dh, Dm) * (H * Dh) ** (-0.5)), 'seg_len': seg}
    _A_in = _A_setup()

    def _B_setup():
        B, S, D, F = (1, 2048, 640, 1536)
        return {'x': _w((lambda *s: torch.randn(*s, dtype=torch.bfloat16) * 0.1)(B, S, D)), 'w1': _w((lambda *s: torch.randn(*s, dtype=torch.bfloat16) * 0.4)(D, F) * D ** (-0.5)), 'b1': _w((lambda *s: torch.randn(*s, dtype=torch.bfloat16) * 0.4)(F) * 0.01), 'w2': _w((lambda *s: torch.randn(*s, dtype=torch.bfloat16) * 0.4)(F, D) * F ** (-0.5)), 'b2': _w((lambda *s: torch.randn(*s, dtype=torch.bfloat16) * 0.4)(D) * 0.01)}
    _B_in = _B_setup()
    return {'A_x': _A_in['x'], 'A_mem0': _A_in['mem0'], 'A_Wq': _A_in['Wq'], 'A_Wk': _A_in['Wk'], 'A_Wv': _A_in['Wv'], 'A_Wo': _A_in['Wo'], 'A_seg_len': _A_in['seg_len'], 'B_x': _B_in['x'], 'B_w1': _B_in['w1'], 'B_b1': _B_in['b1'], 'B_w2': _B_in['w2'], 'B_b2': _B_in['b2']}

def reference(A_x, A_mem0, A_Wq, A_Wk, A_Wv, A_Wo, A_seg_len, B_x, B_w1, B_b1, B_w2, B_b2):

    def _A_ref(x, mem0, Wq, Wk, Wv, Wo, seg_len):
        B, S, Dm = x.shape
        H, Dh = (Wq.shape[1], Wq.shape[2])
        M = mem0.shape[1]
        scale = Dh ** (-0.5)
        n_seg = S // seg_len
        L = M + seg_len
        idx = torch.arange(L)
        mask = (idx[:, None] < M) | (idx[:, None] >= idx[None, :])
        mem = mem0
        outs = []
        for s in range(n_seg):
            xs = x[:, s * seg_len:(s + 1) * seg_len, :].clone()
            h = torch.concatenate([mem, xs], axis=1)
            q = torch.einsum('btd,dhe->bhte', h, Wq)
            k = torch.einsum('btd,dhe->bhte', h, Wk)
            v = torch.einsum('btd,dhe->bhte', h, Wv)
            scores = torch.einsum('bhqe,bhke->bhqk', q, k) * scale
            scores = torch.where(mask[None, None], scores, -1000000000.0)
            p = _softmax(scores, axis=-1)
            o = torch.einsum('bhqk,bhke->bhqe', p, v)
            o = torch.einsum('bhte,hed->btd', o, Wo)
            mem = o[:, :M, :].clone()
            outs.append(o[:, M:, :])
        return torch.concatenate(outs, axis=1)

    def _B_ref(x, w1, b1, w2, b2):
        h = torch.einsum('bsd,df->bsf', x, w1) + b1
        h = _gelu(h)
        return torch.einsum('bsf,fd->bsd', h, w2) + b2
    x = A_x
    xn = _layernorm(x, axis=-1)
    s = _A_ref(xn, A_mem0, A_Wq, A_Wk, A_Wv, A_Wo, A_seg_len)
    g = _silu(_B_ref(xn, B_w1, B_b1, B_w2, B_b2))
    return x + s * g
```

```cpp
#include <hip/hip_runtime.h>
#include <hip/hip_bf16.h>
#include <stdint.h>

#define GAS __attribute__((address_space(1)))
#define LAS __attribute__((address_space(3)))
typedef unsigned short u16;
typedef unsigned v4u __attribute__((ext_vector_type(4)));
typedef unsigned v2u __attribute__((ext_vector_type(2)));
typedef float v4f __attribute__((ext_vector_type(4)));
typedef float v2f __attribute__((ext_vector_type(2)));
typedef short v4s __attribute__((ext_vector_type(4)));
typedef __bf16 v8bf __attribute__((ext_vector_type(8)));
typedef __bf16 v2bf __attribute__((ext_vector_type(2)));
typedef GAS unsigned gu32;
#define RLX_AGENT __ATOMIC_RELAXED, __HIP_MEMORY_SCOPE_AGENT

constexpr int S_ = 2048, DM = 640, NH = 32, DH = 192, HD = NH * DH, MM = 8, SEG = 32, NSEG = 64, FF = 1536;
constexpr unsigned SENT = 0xFFFFFFFFu;

constexpr size_t WS_CTL = 0;
constexpr size_t WS_XN = 4096;
constexpr size_t WS_Q = WS_XN + (size_t)S_ * DM * 2;
constexpr size_t WS_K = WS_Q + (size_t)S_ * HD * 2;
constexpr size_t WS_V = WS_K + (size_t)S_ * HD * 2;
constexpr size_t WS_O = WS_V + (size_t)S_ * HD * 2;
constexpr size_t WS_H1 = WS_O + (size_t)S_ * HD * 2;
constexpr size_t WS_WOF = WS_H1 + (size_t)S_ * FF * 2;
constexpr size_t WS_W2F = WS_WOF + (size_t)HD * DM * 2;
constexpr size_t WS_KM = WS_W2F + (size_t)FF * DM * 2;
constexpr size_t WS_XCH = WS_KM + (size_t)NSEG * NH * 9216;
constexpr size_t XCH_BYTES = 8u << 20;

__device__ __forceinline__ unsigned f2u(float f) { return __builtin_bit_cast(unsigned, f); }
__device__ __forceinline__ float u2f(unsigned u) { return __builtin_bit_cast(float, u); }
__device__ __forceinline__ v4f mfma32(v4u a, v4u b, v4f c) {
  return __builtin_amdgcn_mfma_f32_16x16x32_bf16(__builtin_bit_cast(v8bf, a), __builtin_bit_cast(v8bf, b), c, 0, 0, 0);
}
__device__ __forceinline__ v4f mfma16(v2u a, v2u b, v4f c) {
  return mfma32((v4u){a.x, a.y, 0u, 0u}, (v4u){b.x, b.y, 0u, 0u}, c);
}
__device__ __forceinline__ unsigned pk2(float lo, float hi) {
  v2f v = {lo, hi};
  return __builtin_bit_cast(unsigned, __builtin_convertvector(v, v2bf));
}
__device__ __forceinline__ float bfr(float x) {
  return u2f(pk2(x, 0.f) << 16);
}
__device__ __forceinline__ float bf_lo(unsigned w) { return u2f(w << 16); }
__device__ __forceinline__ float bf_hi(unsigned w) { return u2f(w & 0xffff0000u); }
__device__ __forceinline__ float bf2f(u16 h) { return u2f((unsigned)h << 16); }
__device__ __forceinline__ unsigned umax3(unsigned a, unsigned b, unsigned c) { return max(max(a, b), c); }

__device__ __forceinline__ v4u tr_frag(const LAS unsigned char* img, int stride, int k0, int colp, int lane) {
  const int g = lane >> 4, q = (lane & 15) >> 2;
  const LAS unsigned char* a0 = img + (k0 + 8 * g + q) * stride + colp * 2;
  v4s lo = __builtin_amdgcn_ds_read_tr16_b64_v4i16((LAS v4s*)a0);
  v4s hi = __builtin_amdgcn_ds_read_tr16_b64_v4i16((LAS v4s*)(a0 + 4 * stride));
  v2u l = __builtin_bit_cast(v2u, lo), h = __builtin_bit_cast(v2u, hi);
  return (v4u){l.x, l.y, h.x, h.y};
}

__global__ void __launch_bounds__(256) prologue_kernel(const u16* __restrict__ x, const u16* __restrict__ Wo, const u16* __restrict__ w2,
                                                       unsigned char* ws, unsigned char* xch) {
  __shared__ __attribute__((aligned(16))) unsigned char lds[32 * 1296];
  const int b = blockIdx.x, tid = threadIdx.x, lane = tid & 63, wave = tid >> 6;
  if (b < 512) {
    const int row = b * 4 + wave;
    const unsigned* xr = (const unsigned*)(x + (size_t)row * DM);
    float v[10];
    float sum = 0.f;
#pragma unroll
    for (int i = 0; i < 5; ++i) { unsigned w = xr[lane + 64 * i]; v[2 * i] = bf_lo(w); v[2 * i + 1] = bf_hi(w); sum += v[2 * i] + v[2 * i + 1]; }
#pragma unroll
    for (int m = 1; m < 64; m <<= 1) sum += __shfl_xor(sum, m);
    const float mean = sum * (1.f / DM);
    float sq = 0.f;
#pragma unroll
    for (int i = 0; i < 10; ++i) { float d = v[i] - mean; sq += d * d; }
#pragma unroll
    for (int m = 1; m < 64; m <<= 1) sq += __shfl_xor(sq, m);
    const float mb = bfr(mean), vb = bfr(sq * (1.f / DM));
    const float den = bfr(sqrtf(bfr(vb + 1e-5f)));
    unsigned* xn = (unsigned*)(ws + WS_XN);
#pragma unroll
    for (int i = 0; i < 5; ++i) {
      const int n = 2 * (lane + 64 * i);
      const float a = bfr(v[2 * i] - mb) / den, c = bfr(v[2 * i + 1] - mb) / den;
      const size_t e = ((size_t)(row >> 4) * (DM / 32) + (n >> 5)) * 512 + (((n >> 3) & 3) * 16 + (row & 15)) * 8 + (n & 7);
      xn[e >> 1] = pk2(a, c);
    }
  } else if (b < 512 + 240) {
    const int ks = b - 512;
    const u16* src; unsigned char* dst; int kstep, nks;
    if (ks < 192) { src = Wo + (size_t)ks * 32 * DM; dst = ws + WS_WOF; kstep = ks; nks = 192; }
    else { src = w2 + (size_t)(ks - 192) * 32 * DM; dst = ws + WS_W2F; kstep = ks - 192; nks = 48; }
    for (int c = tid; c < 32 * 80; c += 256) { const int r = c / 80, ch = c % 80;
      *(LAS v4u*)((LAS unsigned char*)lds + r * 1296 + ch * 16) = *(const v4u*)(src + (size_t)r * DM + ch * 8); }
    __syncthreads();
    for (int nt = wave; nt < 40; nt += 4) {
      v4u f = tr_frag((LAS unsigned char*)lds, 1296, 0, nt * 16 + 4 * (lane & 3), lane);
      *(v4u*)(dst + ((size_t)nt * nks + kstep) * 1024 + lane * 16) = f;
    }
  } else if (b == 752) {
    unsigned long long best[2] = {~0ull, ~0ull};
    __amdgpu_buffer_rsrc_t r = __builtin_amdgcn_make_buffer_rsrc(xch, 0, (int)XCH_BYTES, 0x00020000);
    const unsigned base = ((unsigned)wave << 21) + (2u << 20) - 16384u + lane * 16;
    for (int rep = 0; rep < 6; ++rep) {
#pragma unroll
      for (int par = 0; par < 2; ++par) {
        unsigned long long t0, t1;
        asm volatile("s_waitcnt vmcnt(0)\n\ts_memtime %0\n\ts_waitcnt lgkmcnt(0)" : "=s"(t0) :: "memory");
        __builtin_amdgcn_raw_buffer_store_b128((v4u){1u, 2u, 3u, 4u}, r, base + par * 8192u, 0, 16);
        asm volatile("s_waitcnt vmcnt(0)\n\ts_memtime %0\n\ts_waitcnt lgkmcnt(0)" : "=s"(t1) :: "memory");
        if (t1 - t0 < best[par]) best[par] = t1 - t0;
      }
    }
    if (lane == 0) ((unsigned*)(ws + WS_CTL))[16 + wave] = best[1] < best[0] ? 1u : 0u;
    if (tid == 0) ((unsigned*)(ws + WS_CTL))[0] = 0u;
  }
}

__device__ __forceinline__ float gelu_tanh(float x) {
  const float u = 0.7978845608028654f * (x + 0.044715f * x * x * x);
  const float e = __expf(2.f * u);
  const float t = 1.f - 2.f / (e + 1.f);
  return 0.5f * x * (1.f + t);
}

constexpr int PG_LDS = 3 * 40960;
__global__ void __launch_bounds__(512, 2) proj_kernel(const u16* __restrict__ Wq, const u16* __restrict__ Wk, const u16* __restrict__ Wv,
                                                      const u16* __restrict__ w1, const u16* __restrict__ b1, unsigned char* ws, int nsplit) {
  extern __shared__ __attribute__((aligned(16))) unsigned char lds_[];
  LAS unsigned char* lds = (LAS unsigned char*)lds_;
  const int tid = threadIdx.x, lane = tid & 63, wave = __builtin_amdgcn_readfirstlane(tid >> 6);
  const int grp = blockIdx.x / nsplit, part = blockIdx.x % nsplit;
  const u16* W; int ldw, n0; unsigned char* outp; int ncb; bool ffn = false;
  if (grp < 72) { const int m = grp / 24; W = m == 0 ? Wk : (m == 1 ? Wv : Wq); ldw = HD; n0 = (grp % 24) * 256;
                  outp = ws + (m == 0 ? WS_K : (m == 1 ? WS_V : WS_Q)); ncb = HD / 32; }
  else { W = w1; ldw = FF; n0 = (grp - 72) * 256; outp = ws + WS_H1; ncb = FF / 32; ffn = true; }
  v4u wf[20][2];
#pragma unroll
  for (int rd = 0; rd < 4; ++rd) {
    for (int c = tid; c < 160 * 32; c += 512) { const int r = c >> 5, ch = c & 31;
      *(LAS v4u*)(lds + r * 528 + ch * 16) = *(const v4u*)(W + (size_t)(rd * 160 + r) * ldw + n0 + ch * 8); }
    __syncthreads();
#pragma unroll
    for (int kk = 0; kk < 5; ++kk)
#pragma unroll
      for (int t = 0; t < 2; ++t) wf[rd * 5 + kk][t] = tr_frag(lds, 528, kk * 32, wave * 32 + 8 * (lane & 3) + 4 * t, lane);
    __syncthreads();
  }
  float bias[8];
  if (ffn) {
#pragma unroll
    for (int i = 0; i < 8; ++i) bias[i] = bf2f(b1[n0 + wave * 32 + 8 * (lane >> 4) + i]);
  } else {
#pragma unroll
    for (int i = 0; i < 8; ++i) bias[i] = 0.f;
  }
  const int s_begin = (NSEG * part) / nsplit, s_end = (NSEG * (part + 1)) / nsplit;
  const unsigned char* xn = ws + WS_XN;
  auto stage = [&](int s, int buf) {
#pragma unroll
    for (int i = 0; i < 5; ++i) { const int blk = wave * 5 + i;
      __builtin_amdgcn_global_load_lds((const GAS unsigned*)(xn + (size_t)s * 40960 + blk * 1024 + lane * 16), (LAS unsigned*)(lds + buf * 40960 + blk * 1024), 16, 0, 0); }
  };
  stage(s_begin, 0);
  if (s_begin + 1 < s_end) stage(s_begin + 1, 1);
  int buf = 0;
  if (s_begin + 1 < s_end) asm volatile("s_waitcnt vmcnt(5)" ::: "memory"); else asm volatile("s_waitcnt vmcnt(0)" ::: "memory");
  for (int s = s_begin; s < s_end; ++s) {
    __builtin_amdgcn_s_barrier();
    asm volatile("" ::: "memory");
    int nb = buf + 2; if (nb >= 3) nb -= 3;
    if (s + 2 < s_end) stage(s + 2, nb);
    v4f acc[2][2];
#pragma unroll
    for (int a = 0; a < 2; ++a) { acc[a][0] = (v4f){0, 0, 0, 0}; acc[a][1] = (v4f){0, 0, 0, 0}; }
    const LAS unsigned char* A = lds + buf * 40960 + lane * 16;
#pragma unroll
    for (int kk = 0; kk < 20; ++kk) {
      const v4u x0 = *(const LAS v4u*)(A + kk * 1024), x1 = *(const LAS v4u*)(A + 20480 + kk * 1024);
      acc[0][0] = mfma32(wf[kk][0], x0, acc[0][0]); acc[0][1] = mfma32(wf[kk][1], x0, acc[0][1]);
      acc[1][0] = mfma32(wf[kk][0], x1, acc[1][0]); acc[1][1] = mfma32(wf[kk][1], x1, acc[1][1]);
    }
#pragma unroll
    for (int a = 0; a < 2; ++a) {
      float o[8];
#pragma unroll
      for (int r = 0; r < 4; ++r) { o[r] = acc[a][0][r]; o[4 + r] = acc[a][1][r]; }
      if (ffn) {
#pragma unroll
        for (int i = 0; i < 8; ++i) o[i] = gelu_tanh(bfr(bfr(o[i]) + bias[i]));
      }
      v4u pkd = {pk2(o[0], o[1]), pk2(o[2], o[3]), pk2(o[4], o[5]), pk2(o[6], o[7])};
      *(v4u*)(outp + ((size_t)(s * 2 + a) * ncb + (n0 >> 5) + wave) * 1024 + lane * 16) = pkd;
    }
    if (s + 2 < s_end) asm volatile("s_waitcnt vmcnt(7)" ::: "memory"); else asm volatile("s_waitcnt vmcnt(2)" ::: "memory");
    buf = buf + 1; if (buf >= 3) buf = 0;
  }
}

struct ChainArgs {
  const u16 *Wq, *Wk, *Wv, *Wo, *mem0;
  unsigned char* ws;
  unsigned char* xch;
};
constexpr int CL_MEM = 0, CL_VX = 10240, CL_PB = 22528, CL_V2P = 24576, CL_FLAG = 25600, CL_STG = 26624;
constexpr int CH_LDS = CL_STG + 640 * 96;
constexpr unsigned SPIN_LIMIT = 400000u;

__device__ __forceinline__ v4u ldx(__amdgpu_buffer_rsrc_t r, unsigned voff, unsigned soff) { return __builtin_amdgcn_raw_buffer_load_b128(r, voff, soff, 16); }
__device__ __forceinline__ void stx16(__amdgpu_buffer_rsrc_t r, unsigned voff, v4u v) { __builtin_amdgcn_raw_buffer_store_b128(v, r, voff, 0, 16); }
__device__ __forceinline__ void stx8(__amdgpu_buffer_rsrc_t r, unsigned voff, unsigned soff, v2u v) { __builtin_amdgcn_raw_buffer_store_b64(v, r, voff, soff, 16); }
__device__ __forceinline__ void stx4(__amdgpu_buffer_rsrc_t r, unsigned voff, unsigned soff, unsigned v) { __builtin_amdgcn_raw_buffer_store_b32(v, r, voff, soff, 16); }
__device__ __forceinline__ unsigned xmap(unsigned o, unsigned par) { return ((o >> 13) << 14) + (par << 13) + (o & 8191u); }
__device__ __forceinline__ float dpp_shl8(float v) {
  return u2f((unsigned)__builtin_amdgcn_update_dpp(0, (int)f2u(v), 0x108, 0xf, 0xf, true));
}
#define POLL_FAIL(spins, dead, abortw) \
  ((++(spins) > SPIN_LIMIT) ? (__hip_atomic_store((abortw), 1u, RLX_AGENT), (dead) = 1, true) \
   : ((((spins) & 255u) == 0u && __builtin_amdgcn_readfirstlane(__hip_atomic_load((abortw), RLX_AGENT)) != 0u) ? ((dead) = 1, true) : false))

template <int P0, int P1>
__device__ __forceinline__ v4f stageA(const v4u (&w)[2 * (P1 - P0)], const LAS unsigned char* memb) {
  v4f a0 = {0, 0, 0, 0}, a1 = {0, 0, 0, 0};
#pragma unroll
  for (int pp = P0; pp < P1; ++pp) {
    const v4u x = *(const LAS v4u*)(memb + pp * 1024);
    a0 = mfma32(w[2 * (pp - P0)], x, a0);
    a1 = mfma32(w[2 * (pp - P0) + 1], x, a1);
  }
  v4f d;
#pragma unroll
  for (int r = 0; r < 4; ++r) d[r] = a0[r] + dpp_shl8(a1[r]);
  return d;
}

__global__ void __launch_bounds__(512, 2) chain_kernel(ChainArgs a) {
  extern __shared__ __attribute__((aligned(16))) unsigned char lds_[];
  LAS unsigned char* lds = (LAS unsigned char*)lds_;
  const int b = blockIdx.x;
  if ((b & 7) >= 4) return;
  const int tid = threadIdx.x, lane = tid & 63, wave = __builtin_amdgcn_readfirstlane(tid >> 6);
  const int cu = (b & 3) * 32 + (b >> 3), h = cu >> 2, j = cu & 3;
  const int g = lane >> 4, r16 = lane & 15;
  gu32* ctl = (gu32*)(a.ws + WS_CTL);
  gu32* abortw = ctl;
  const unsigned par3 = ctl[16], par1 = ctl[17], par2a = ctl[18], par2b = ctl[19];
  const __amdgpu_buffer_rsrc_t rx = __builtin_amdgcn_make_buffer_rsrc(a.xch, 0, (int)XCH_BYTES, 0x00020000);
  const __amdgpu_buffer_rsrc_t rm0 = __builtin_amdgcn_make_buffer_rsrc((void*)a.mem0, 0, MM * DM * 2, 0x00020000);
  const unsigned char* Kf = a.ws + WS_K; const unsigned char* Vf = a.ws + WS_V;
  unsigned char* KM = a.ws + WS_KM;
  int dead = 0;
  if (tid == 0) *(LAS unsigned*)(lds + CL_FLAG) = 0u;

  const int mat = wave / 3, sub = wave % 3;
  v4u wA[20];
  v4u wX[12];
#pragma unroll
  for (int m = 0; m < 3; ++m) {
    const u16* W = m == 0 ? a.Wq : (m == 1 ? a.Wk : a.Wv);
    for (int c = tid; c < 640 * 6; c += 512) { const int r = c / 6, ch = c % 6;
      *(LAS v4u*)(lds + CL_STG + r * 96 + ch * 16) = *(const v4u*)(W + (size_t)r * HD + h * DH + 48 * j + ch * 8); }
    __syncthreads();
    if (mat == m) {
#pragma unroll
      for (int kk = 0; kk < 20; ++kk) wA[kk] = tr_frag(lds + CL_STG, 96, kk * 32, sub * 16 + 4 * (lane & 3), lane);
      if (m == 2) {
#pragma unroll
        for (int kk = 0; kk < 10; ++kk) wX[kk] = tr_frag(lds + CL_STG, 96, (sub * 10 + kk) * 32, 32 + 4 * (lane & 3), lane);
      }
    }
    __syncthreads();
  }
  for (int c = tid; c < 192 * 20; c += 512) { const int r = c / 20, ch = c % 20;
    *(LAS v4u*)(lds + CL_STG + r * 320 + ch * 16) = *(const v4u*)(a.Wo + (size_t)(h * DH + r) * DM + 160 * j + ch * 8); }
  for (int c = tid; c < 12 * 64; c += 512) { const int blk = c >> 6, l = c & 63;
    *(LAS v4u*)(lds + CL_VX + c * 16) = *(const v4u*)(Vf + ((size_t)(blk / 6) * (HD / 32) + h * 6 + blk % 6) * 1024 + l * 16); }
  __syncthreads();
  if (wave >= 1 && wave <= 5) {
#pragma unroll
    for (int t = 0; t < 2; ++t)
#pragma unroll
      for (int kk = 0; kk < 6; ++kk) wX[t * 6 + kk] = tr_frag(lds + CL_STG, 320, kk * 32, ((wave - 1) * 2 + t) * 16 + 4 * (lane & 3), lane);
  }

  auto x1off = [&](int m, int sb) -> unsigned {
    const int e0 = 48 * j + 16 * sb;
    if (m < 2) return (unsigned)((e0 >> 5) * 1024 + ((((e0 & 31) >> 3) + (g >> 1)) * 16 + (r16 & 7) + 8 * m) * 16 + (g & 1) * 8);
    return (unsigned)((6 + (e0 >> 6)) * 1024 + ((((e0 & 31) >> 3) + (g >> 1)) * 16 + (r16 & 7) + 8 * ((e0 & 63) >> 5)) * 16 + (g & 1) * 8);
  };
  const unsigned pubo = x1off(mat, sub);
  const unsigned X1B = 2u << 20, X2B0 = 4u << 20, X2B1 = 6u << 20;
  const unsigned pub0 = X1B + xmap((unsigned)(h * 9216) + pubo, par1), pub1 = X1B + xmap((unsigned)((32 + h) * 9216) + pubo, par1);
  const LAS unsigned char* memb = lds + CL_MEM + lane * 16;
  const float cexp = 0.07216878364870322f * 1.4426950408889634f;

  if (wave == 0) {
    v4u kx[12];
#pragma unroll
    for (int i = 0; i < 12; ++i) kx[i] = *(const v4u*)(Kf + ((size_t)(i / 6) * (HD / 32) + h * 6 + i % 6) * 1024 + lane * 16);
    unsigned qo[2][6];
#pragma unroll
    for (int sl = 0; sl < 2; ++sl)
#pragma unroll
      for (int kk = 0; kk < 6; ++kk) qo[sl][kk] = X1B + xmap((unsigned)((sl * 32 + h) * 9216 + kk * 1024), par1);
    for (int s = 0; s < NSEG; ++s) {
      const int sl = s & 1;
      __builtin_amdgcn_s_barrier();
      asm volatile("" ::: "memory");
      if (r16 < 8) stx8(rx, sl ? pub0 : pub1, 0, (v2u){SENT, SENT});
      v4f d = stageA<0, 10>(wA, memb);
      v2u pk = {min(pk2(d[0], d[1]), 0xFFFFFFFEu), pk2(d[2], d[3])};
      if (r16 < 8) stx8(rx, sl ? pub1 : pub0, 0, pk);
      if (s == NSEG - 1) break;
      v4u x[6];
      unsigned spins = 0;
      for (;;) {
#pragma unroll
        for (int kk = 0; kk < 6; ++kk) x[kk] = ldx(rx, lane * 16, sl ? qo[1][kk] : qo[0][kk]);
        unsigned m = umax3(x[0].x, x[0].z, x[1].x);
        m = umax3(m, x[1].z, x[2].x); m = umax3(m, x[2].z, x[3].x); m = umax3(m, x[3].z, x[4].x); m = umax3(m, x[4].z, x[5].x); m = max(m, x[5].z);
        if (__builtin_amdgcn_ballot_w64(m == SENT) == 0ull) break;
        if (dead || POLL_FAIL(spins, dead, abortw)) break;
      }
      v4f t0 = {0, 0, 0, 0}, t1 = t0, t2 = t0;
#pragma unroll
      for (int kk = 0; kk < 6; ++kk) { t0 = mfma32(x[kk], x[kk], t0); t1 = mfma32(kx[kk], x[kk], t1); t2 = mfma32(kx[6 + kk], x[kk], t2); }
      if (g < 2) t0 = (v4f){-INFINITY, -INFINITY, -INFINITY, -INFINITY};
      float mx = fmaxf(fmaxf(fmaxf(t0[0], t0[1]), fmaxf(t0[2], t0[3])), fmaxf(fmaxf(fmaxf(t1[0], t1[1]), fmaxf(t1[2], t1[3])), fmaxf(fmaxf(t2[0], t2[1]), fmaxf(t2[2], t2[3]))));
      { auto r = __builtin_amdgcn_permlane16_swap(f2u(mx), f2u(mx), false, false);
        mx = fmaxf(u2f(r[0]), u2f(r[1])); }
      { auto r = __builtin_amdgcn_permlane32_swap(f2u(mx), f2u(mx), false, false);
        mx = fmaxf(u2f(r[0]), u2f(r[1])); }
      const float mc = mx * cexp;
      float p[12];
#pragma unroll
      for (int r = 0; r < 4; ++r) {
        p[r] = __builtin_amdgcn_exp2f(t0[r] * cexp - mc); p[4 + r] = __builtin_amdgcn_exp2f(t1[r] * cexp - mc); p[8 + r] = __builtin_amdgcn_exp2f(t2[r] * cexp - mc); }
      v4u px = {pk2(p[4], p[5]), pk2(p[6], p[7]), pk2(p[8], p[9]), pk2(p[10], p[11])};
      *(LAS v4u*)(lds + CL_PB + lane * 16) = px;
      float l = ((p[0] + p[1]) + (p[2] + p[3])) + ((p[4] + p[5]) + (p[6] + p[7])) + ((p[8] + p[9]) + (p[10] + p[11]));
      { auto r = __builtin_amdgcn_permlane16_swap(f2u(l), f2u(l), false, false);
        l = u2f(r[0]) + u2f(r[1]); }
      { auto r = __builtin_amdgcn_permlane32_swap(f2u(l), f2u(l), false, false);
        l = u2f(r[0]) + u2f(r[1]); }
      v4u pm = {pk2(p[0], p[1]), pk2(p[2], p[3]), f2u(__builtin_amdgcn_rcpf(l)), 0u};
      *(LAS v4u*)(lds + CL_PB + 1024 + lane * 16) = pm;
      asm volatile("s_waitcnt lgkmcnt(0)" ::: "memory");
      __builtin_amdgcn_s_barrier();
      asm volatile("" ::: "memory");
#pragma unroll
      for (int i = 0; i < 12; ++i) kx[i] = *(const v4u*)(Kf + ((size_t)((s + 1) * 2 + i / 6) * (HD / 32) + h * 6 + i % 6) * 1024 + lane * 16);
    }
  } else if (wave <= 5) {
    v4u yf[2];
    auto computeY = [&]() {
      v4f y[2][2];
#pragma unroll
      for (int t = 0; t < 2; ++t) { y[t][0] = (v4f){0, 0, 0, 0}; y[t][1] = (v4f){0, 0, 0, 0}; }
#pragma unroll
      for (int kk = 0; kk < 6; ++kk) {
        const v4u v0 = *(const LAS v4u*)(lds + CL_VX + kk * 1024 + lane * 16), v1 = *(const LAS v4u*)(lds + CL_VX + (6 + kk) * 1024 + lane * 16);
#pragma unroll
        for (int t = 0; t < 2; ++t) { y[t][0] = mfma32(v0, wX[t * 6 + kk], y[t][0]); y[t][1] = mfma32(v1, wX[t * 6 + kk], y[t][1]); }
      }
#pragma unroll
      for (int t = 0; t < 2; ++t) yf[t] = (v4u){pk2(y[t][0][0], y[t][0][1]), pk2(y[t][0][2], y[t][0][3]), pk2(y[t][1][0], y[t][1][1]), pk2(y[t][1][2], y[t][1][3])};
    };
    computeY();
    unsigned vo[2][3];
#pragma unroll
    for (int sl = 0; sl < 2; ++sl)
#pragma unroll
      for (int pp = 0; pp < 3; ++pp) vo[sl][pp] = X1B + xmap((unsigned)((sl * 32 + h) * 9216 + (6 + pp) * 1024), par1);
    unsigned x2o[2];
#pragma unroll
    for (int t = 0; t < 2; ++t) { const int T = 10 * j + 2 * (wave - 1) + t;
      x2o[t] = (unsigned)(((2 * T + (g >> 1)) << 14) + h * 256 + ((r16 & 7) * 2 + (g & 1)) * 16); }
    const unsigned x2s0 = X2B0 + (par2a << 13), x2s1 = X2B1 + (par2b << 13);
    const int blk0 = (wave - 1) * 2;
    const unsigned m0o = (unsigned)(((r16 & 7) * DM + 32 * (r16 >> 3) + 8 * g) * 2);
    const unsigned kmo = pubo;
    for (int s = 0; s < NSEG; ++s) {
      const int sl = s & 1;
      v4u m0, m1;
      if (s == 0) {
        m0 = __builtin_amdgcn_raw_buffer_load_b128(rm0, m0o, blk0 * 128, 0);
        m1 = __builtin_amdgcn_raw_buffer_load_b128(rm0, m0o, blk0 * 128 + 128, 0);
      } else {
        const unsigned o0 = xmap((unsigned)(s * 10240 + blk0 * 1024), par3), o1 = xmap((unsigned)(s * 10240 + blk0 * 1024 + 1024), par3);
        unsigned spins = 0;
        for (;;) {
          m0 = ldx(rx, lane * 16, o0); m1 = ldx(rx, lane * 16, o1);
          unsigned m = umax3(m0.x, m0.y, m0.z); m = umax3(m, m0.w, m1.x); m = umax3(m, m1.y, m1.z); m = max(m, m1.w);
          if (__builtin_amdgcn_ballot_w64(m == SENT) == 0ull) break;
          if (dead || POLL_FAIL(spins, dead, abortw)) break;
        }
      }
      *(LAS v4u*)(lds + CL_MEM + blk0 * 1024 + lane * 16) = m0;
      *(LAS v4u*)(lds + CL_MEM + blk0 * 1024 + 1024 + lane * 16) = m1;
      asm volatile("s_waitcnt lgkmcnt(0)" ::: "memory");
      __builtin_amdgcn_s_barrier();
      asm volatile("" ::: "memory");
      if (r16 < 8) stx8(rx, sl ? pub0 : pub1, 0, (v2u){SENT, SENT});
      v4f d = stageA<0, 10>(wA, memb);
      v2u pk = {min(pk2(d[0], d[1]), 0xFFFFFFFEu), pk2(d[2], d[3])};
      if (r16 < 8) {
        stx8(rx, sl ? pub1 : pub0, 0, pk);
        if (mat == 1) *(v2u*)(KM + (size_t)(s * NH + h) * 9216 + kmo) = pk;
      }
      if (s == NSEG - 1) break;
      v4u x[3];
      {
        unsigned spins = 0;
        for (;;) {
#pragma unroll
          for (int pp = 0; pp < 3; ++pp) x[pp] = ldx(rx, lane * 16, sl ? vo[1][pp] : vo[0][pp]);
          unsigned m = umax3(x[0].x, x[0].z, x[1].x); m = umax3(m, x[1].z, x[2].x); m = max(m, x[2].z);
          if (__builtin_amdgcn_ballot_w64(m == SENT) == 0ull) break;
          if (dead || POLL_FAIL(spins, dead, abortw)) break;
        }
      }
      v2u uf[2];
#pragma unroll
      for (int t = 0; t < 2; ++t) {
        v4f u0 = {0, 0, 0, 0}, u1 = u0;
#pragma unroll
        for (int pp = 0; pp < 3; ++pp) { u0 = mfma32(x[pp], wX[t * 6 + 2 * pp], u0); u1 = mfma32(x[pp], wX[t * 6 + 2 * pp + 1], u1); }
        v4f u = g < 2 ? u0 : u1;
        uf[t] = (v2u){pk2(u[0], u[1]), pk2(u[2], u[3])};
      }
      __builtin_amdgcn_s_barrier();
      asm volatile("" ::: "memory");
      const v4u px = *(const LAS v4u*)(lds + CL_PB + lane * 16);
      const v4u pm = *(const LAS v4u*)(lds + CL_PB + 1024 + (lane | 32) * 16);
      const float linv = u2f(pm.z);
#pragma unroll
      for (int t = 0; t < 2; ++t) {
        v4f o = mfma32(yf[t], px, (v4f){0, 0, 0, 0});
        o = mfma16(uf[t], (v2u){pm.x, pm.y}, o);
        v4u ov = {min(f2u(o[0] * linv), 0xFFFFFFFEu), f2u(o[1] * linv),
                  min(f2u(o[2] * linv), 0xFFFFFFFEu), f2u(o[3] * linv)};
        if (r16 < 8) stx16(rx, x2o[t] + (sl ? x2s1 : x2s0), ov);
      }
      computeY();
    }
  } else {
    const unsigned pubo2 = x1off(2, 2);
    const unsigned pub20 = X1B + xmap((unsigned)(h * 9216) + pubo2, par1), pub21 = X1B + xmap((unsigned)((32 + h) * 9216) + pubo2, par1);
    const bool red = (wave == 6) && ((cu & 7) < 5);
    const int U = (cu >> 3) * 5 + (cu & 7);
    const unsigned x2s0 = X2B0 + (par2a << 13) + ((unsigned)U << 14), x2s1 = X2B1 + (par2b << 13) + ((unsigned)U << 14);
    const unsigned x3o = (unsigned)((U >> 3) * 1024 + ((U & 3) * 16 + 8 * ((U & 7) >> 2)) * 16 + (r16 >> 1) * 16 + (r16 & 1) * 8 + (lane >> 5) * 4);
    for (int s = 0; s < NSEG; ++s) {
      const int sl = s & 1;
      __builtin_amdgcn_s_barrier();
      asm volatile("" ::: "memory");
      if (r16 < 8) { stx8(rx, sl ? pub0 : pub1, 0, (v2u){SENT, SENT}); if (wave == 7) stx8(rx, sl ? pub20 : pub21, 0, (v2u){SENT, SENT}); }
      v4u vx[6];
      if (s + 1 < NSEG) {
#pragma unroll
        for (int i = 0; i < 6; ++i) vx[i] = *(const v4u*)(Vf + ((size_t)((s + 1) * 2 + (wave - 6)) * (HD / 32) + h * 6 + i) * 1024 + lane * 16);
      }
      if (wave == 6) {
        v4u wh[10];
#pragma unroll
        for (int i = 0; i < 10; ++i) wh[i] = wX[i];
        v4f d2 = stageA<0, 5>(wh, memb);
        *(LAS v4f*)(lds + CL_V2P + lane * 16) = d2;
        asm volatile("s_waitcnt lgkmcnt(0)" ::: "memory");
        if (lane == 0) *(volatile LAS unsigned*)(lds + CL_FLAG) = (unsigned)(s + 1);
      }
      v4f d = stageA<0, 10>(wA, memb);
      v2u pk = {min(pk2(d[0], d[1]), 0xFFFFFFFEu), pk2(d[2], d[3])};
      if (r16 < 8) { stx8(rx, sl ? pub1 : pub0, 0, pk); *(v2u*)(KM + (size_t)(s * NH + h) * 9216 + pubo) = pk; }
      if (wave == 7) {
        v4u wh[10];
#pragma unroll
        for (int i = 0; i < 10; ++i) wh[i] = wX[i];
        v4f d2 = stageA<5, 10>(wh, memb);
        while (*(volatile LAS unsigned*)(lds + CL_FLAG) != (unsigned)(s + 1)) {}
        const v4f o2 = *(const LAS v4f*)(lds + CL_V2P + lane * 16);
        d2 += o2;
        v2u pk2v = {min(pk2(d2[0], d2[1]), 0xFFFFFFFEu), pk2(d2[2], d2[3])};
        if (r16 < 8) { stx8(rx, sl ? pub21 : pub20, 0, pk2v); *(v2u*)(KM + (size_t)(s * NH + h) * 9216 + pubo2) = pk2v; }
      }
      if (s == NSEG - 1) break;
#pragma unroll
      for (int i = 0; i < 6; ++i) *(LAS v4u*)(lds + CL_VX + ((wave - 6) * 6 + i) * 1024 + lane * 16) = vx[i];
      asm volatile("s_waitcnt vmcnt(0) lgkmcnt(0)" ::: "memory");
      __builtin_amdgcn_s_barrier();
      asm volatile("" ::: "memory");
      if (red) {
        const unsigned so = sl ? x2s1 : x2s0;
        v4u x[8];
        unsigned spins = 0;
        for (;;) {
#pragma unroll
          for (int i = 0; i < 8; ++i) x[i] = ldx(rx, lane * 16 + i * 1024, so);
          unsigned m = umax3(x[0].x, x[0].z, x[1].x);
          m = umax3(m, x[1].z, x[2].x); m = umax3(m, x[2].z, x[3].x); m = umax3(m, x[3].z, x[4].x); m = umax3(m, x[4].z, x[5].x);
          m = umax3(m, x[5].z, x[6].x); m = umax3(m, x[6].z, x[7].x); m = max(m, x[7].z);
          if (__builtin_amdgcn_ballot_w64(m == SENT) == 0ull) break;
          if (dead || POLL_FAIL(spins, dead, abortw)) break;
        }
        v4f sm = ((__builtin_bit_cast(v4f, x[0]) + __builtin_bit_cast(v4f, x[1])) + (__builtin_bit_cast(v4f, x[2]) + __builtin_bit_cast(v4f, x[3]))) +
                 ((__builtin_bit_cast(v4f, x[4]) + __builtin_bit_cast(v4f, x[5])) + (__builtin_bit_cast(v4f, x[6]) + __builtin_bit_cast(v4f, x[7])));
        float va, vb;
        { auto r = __builtin_amdgcn_permlane32_swap(f2u(sm[0]), f2u(sm[2]), false, false);
          va = u2f(r[0]) + u2f(r[1]); }
        { auto r = __builtin_amdgcn_permlane32_swap(f2u(sm[1]), f2u(sm[3]), false, false);
          vb = u2f(r[0]) + u2f(r[1]); }
        { auto r = __builtin_amdgcn_permlane16_swap(f2u(va), f2u(va), false, false);
          va = u2f(r[0]) + u2f(r[1]); }
        { auto r = __builtin_amdgcn_permlane16_swap(f2u(vb), f2u(vb), false, false);
          vb = u2f(r[0]) + u2f(r[1]); }
        const unsigned w = min(pk2(va, vb), 0xFFFFFFFEu);
        if ((g & 1) == 0) stx4(rx, xmap((unsigned)((s + 1) * 10240) + x3o, par3), 0, w);
#pragma unroll
        for (int i = 0; i < 8; ++i) stx16(rx, lane * 16 + i * 1024 + so, (v4u){SENT, SENT, SENT, SENT});
      }
    }
  }
}

constexpr int AT_VSTRIDE = 400;
__global__ void __launch_bounds__(256) attn_kernel(unsigned char* ws) {
  __shared__ __attribute__((aligned(16))) unsigned char lds_[4 * 48 * AT_VSTRIDE];
  const int tid = threadIdx.x, lane = tid & 63, wave = __builtin_amdgcn_readfirstlane(tid >> 6);
  const int unit = blockIdx.x * 4 + wave, s = unit >> 5, h = unit & 31;
  const int g = lane >> 4, r16 = lane & 15;
  LAS unsigned char* vimg = (LAS unsigned char*)lds_ + wave * 48 * AT_VSTRIDE;
  const unsigned char* Qf = ws + WS_Q; const unsigned char* Kf = ws + WS_K; const unsigned char* Vf = ws + WS_V;
  const unsigned char* km = ws + WS_KM + (size_t)(s * NH + h) * 9216;
#pragma unroll
  for (int pp = 0; pp < 3; ++pp) { const v4u v = *(const v4u*)(km + (6 + pp) * 1024 + lane * 16);
    *(LAS v4u*)(vimg + (r16 & 7) * AT_VSTRIDE + (64 * pp + 32 * (r16 >> 3) + 8 * g) * 2) = v;
    *(LAS v4u*)(vimg + (8 + (r16 & 7)) * AT_VSTRIDE + (64 * pp + 32 * (r16 >> 3) + 8 * g) * 2) = (v4u){0, 0, 0, 0}; }
#pragma unroll
  for (int i = 0; i < 12; ++i) { const int kt = i / 6, kk = i % 6;
    const v4u v = *(const v4u*)(Vf + ((size_t)(s * 2 + kt) * (HD / 32) + h * 6 + kk) * 1024 + lane * 16);
    *(LAS v4u*)(vimg + (16 + kt * 16 + r16) * AT_VSTRIDE + (32 * kk + 8 * g) * 2) = v; }
  v4f t[2][3];
#pragma unroll
  for (int qt = 0; qt < 2; ++qt) { t[qt][0] = (v4f){0, 0, 0, 0}; t[qt][1] = t[qt][0]; t[qt][2] = t[qt][0]; }
#pragma unroll
  for (int kk = 0; kk < 6; ++kk) {
    v4u kmf = *(const v4u*)(km + kk * 1024 + (lane | 8) * 16);
    const v4u k0 = *(const v4u*)(Kf + ((size_t)(s * 2) * (HD / 32) + h * 6 + kk) * 1024 + lane * 16);
    const v4u k1 = *(const v4u*)(Kf + ((size_t)(s * 2 + 1) * (HD / 32) + h * 6 + kk) * 1024 + lane * 16);
#pragma unroll
    for (int qt = 0; qt < 2; ++qt) {
      const v4u q = *(const v4u*)(Qf + ((size_t)(s * 2 + qt) * (HD / 32) + h * 6 + kk) * 1024 + lane * 16);
      t[qt][0] = mfma32(kmf, q, t[qt][0]); t[qt][1] = mfma32(k0, q, t[qt][1]); t[qt][2] = mfma32(k1, q, t[qt][2]);
    }
  }
  const float cexp = 0.07216878364870322f * 1.4426950408889634f;
  v2u pm[2]; v4u px[2];
#pragma unroll
  for (int qt = 0; qt < 2; ++qt) {
    const int qi = qt * 16 + r16;
    float sc[12];
#pragma unroll
    for (int r = 0; r < 4; ++r) {
      sc[r] = g < 2 ? t[qt][0][r] : -INFINITY;
      sc[4 + r] = (4 * g + r <= qi) ? t[qt][1][r] : -INFINITY;
      sc[8 + r] = (16 + 4 * g + r <= qi) ? t[qt][2][r] : -INFINITY;
    }
    float mx = sc[0];
#pragma unroll
    for (int i = 1; i < 12; ++i) mx = fmaxf(mx, sc[i]);
    mx = fmaxf(mx, __shfl_xor(mx, 16)); mx = fmaxf(mx, __shfl_xor(mx, 32));
    const float mc = mx * cexp;
    float p[12], l = 0.f;
#pragma unroll
    for (int i = 0; i < 12; ++i) { p[i] = __builtin_amdgcn_exp2f(sc[i] * cexp - mc); l += p[i]; }
    l += __shfl_xor(l, 16); l += __shfl_xor(l, 32);
    const float li = 1.f / l;
#pragma unroll
    for (int i = 0; i < 12; ++i) p[i] *= li;
    pm[qt] = (v2u){pk2(p[0], p[1]), pk2(p[2], p[3])};
    px[qt] = (v4u){pk2(p[4], p[5]), pk2(p[6], p[7]), pk2(p[8], p[9]), pk2(p[10], p[11])};
  }
  asm volatile("s_waitcnt lgkmcnt(0)" ::: "memory");
  __builtin_amdgcn_wave_barrier();
  unsigned char* Of = ws + WS_O;
  const int q4 = (lane & 15) >> 2, p4 = lane & 3;
#pragma unroll
  for (int kk = 0; kk < 6; ++kk) {
    v4f o[2][2];
#pragma unroll
    for (int tt = 0; tt < 2; ++tt) {
      const int col = 32 * kk + 8 * p4 + 4 * tt;
      const LAS unsigned char* base = vimg + (4 * g + q4) * AT_VSTRIDE + col * 2;
      const v2u vm = __builtin_bit_cast(v2u, __builtin_amdgcn_ds_read_tr16_b64_v4i16((LAS v4s*)base));
      const v2u v0 = __builtin_bit_cast(v2u, __builtin_amdgcn_ds_read_tr16_b64_v4i16((LAS v4s*)(base + 16 * AT_VSTRIDE)));
      const v2u v1 = __builtin_bit_cast(v2u, __builtin_amdgcn_ds_read_tr16_b64_v4i16((LAS v4s*)(base + 32 * AT_VSTRIDE)));
      const v4u vx = {v0.x, v0.y, v1.x, v1.y};
#pragma unroll
      for (int qt = 0; qt < 2; ++qt) { o[qt][tt] = mfma32(vx, px[qt], (v4f){0, 0, 0, 0}); o[qt][tt] = mfma16(vm, pm[qt], o[qt][tt]); }
    }
#pragma unroll
    for (int qt = 0; qt < 2; ++qt) {
      v4u pkd = {pk2(o[qt][0][0], o[qt][0][1]), pk2(o[qt][0][2], o[qt][0][3]), pk2(o[qt][1][0], o[qt][1][1]), pk2(o[qt][1][2], o[qt][1][3])};
      *(v4u*)(Of + ((size_t)(s * 2 + qt) * (HD / 32) + h * 6 + kk) * 1024 + lane * 16) = pkd;
    }
  }
}

__global__ void __launch_bounds__(256) out_kernel(const u16* __restrict__ x, const u16* __restrict__ b2, unsigned char* ws, u16* __restrict__ out) {
  __shared__ __attribute__((aligned(16))) float red[2][3][20][256];
  const int tid = threadIdx.x, lane = tid & 63, wave = __builtin_amdgcn_readfirstlane(tid >> 6);
  const int bm = blockIdx.x >> 3, bn = blockIdx.x & 7;
  const unsigned char* Of = ws + WS_O + (size_t)bm * 4 * 192 * 1024 + lane * 16;
  const unsigned char* Hf = ws + WS_H1 + (size_t)bm * 4 * 48 * 1024 + lane * 16;
  const unsigned char* Wf = ws + WS_WOF + (size_t)bn * 5 * 192 * 1024 + lane * 16;
  const unsigned char* W2 = ws + WS_W2F + (size_t)bn * 5 * 48 * 1024 + lane * 16;
  v4f acc[4][5], acg[4][5];
#pragma unroll
  for (int m = 0; m < 4; ++m)
#pragma unroll
    for (int n = 0; n < 5; ++n) { acc[m][n] = (v4f){0, 0, 0, 0}; acg[m][n] = (v4f){0, 0, 0, 0}; }
#pragma unroll 2
  for (int k = wave * 48; k < wave * 48 + 48; ++k) {
    v4u af[4], wf[5];
#pragma unroll
    for (int m = 0; m < 4; ++m) af[m] = *(const v4u*)(Of + ((size_t)m * 192 + k) * 1024);
#pragma unroll
    for (int n = 0; n < 5; ++n) wf[n] = *(const v4u*)(Wf + ((size_t)n * 192 + k) * 1024);
#pragma unroll
    for (int m = 0; m < 4; ++m)
#pragma unroll
      for (int n = 0; n < 5; ++n) acc[m][n] = mfma32(wf[n], af[m], acc[m][n]);
  }
#pragma unroll 2
  for (int k = wave * 12; k < wave * 12 + 12; ++k) {
    v4u af[4], wf[5];
#pragma unroll
    for (int m = 0; m < 4; ++m) af[m] = *(const v4u*)(Hf + ((size_t)m * 48 + k) * 1024);
#pragma unroll
    for (int n = 0; n < 5; ++n) wf[n] = *(const v4u*)(W2 + ((size_t)n * 48 + k) * 1024);
#pragma unroll
    for (int m = 0; m < 4; ++m)
#pragma unroll
      for (int n = 0; n < 5; ++n) acg[m][n] = mfma32(wf[n], af[m], acg[m][n]);
  }
  if (wave > 0) {
#pragma unroll
    for (int m = 0; m < 4; ++m)
#pragma unroll
      for (int n = 0; n < 5; ++n) { *(v4f*)&red[0][wave - 1][m * 5 + n][lane * 4] = acc[m][n]; *(v4f*)&red[1][wave - 1][m * 5 + n][lane * 4] = acg[m][n]; }
  }
  __syncthreads();
  if (wave == 0) {
    const int g = lane >> 4, r16 = lane & 15;
#pragma unroll
    for (int m = 0; m < 4; ++m)
#pragma unroll
      for (int n = 0; n < 5; ++n) {
        v4f sa = acc[m][n], sg = acg[m][n];
#pragma unroll
        for (int w = 0; w < 3; ++w) { sa += *(const v4f*)&red[0][w][m * 5 + n][lane * 4]; sg += *(const v4f*)&red[1][w][m * 5 + n][lane * 4]; }
        const int row = bm * 64 + m * 16 + r16, col = bn * 80 + n * 16 + 4 * g;
        const v2u xv = *(const v2u*)(x + (size_t)row * DM + col);
        const v2u bv = *(const v2u*)(b2 + col);
        float xs[4] = {bf_lo(xv.x), bf_hi(xv.x), bf_lo(xv.y), bf_hi(xv.y)};
        float bs[4] = {bf_lo(bv.x), bf_hi(bv.x), bf_lo(bv.y), bf_hi(bv.y)};
        float o[4];
#pragma unroll
        for (int r = 0; r < 4; ++r) {
          const float hh = bfr(bfr(sg[r]) + bs[r]);
          const float gg = bfr(hh / (1.f + __expf(-hh)));
          o[r] = xs[r] + bfr(bfr(sa[r]) * gg);
        }
        *(v2u*)(out + (size_t)row * DM + col) = (v2u){pk2(o[0], o[1]), pk2(o[2], o[3])};
      }
  }
}

extern "C" void kernel_launch(void* const* d_in, const int* in_sizes, int n_in, void* d_out, int out_size, void* d_ws, size_t ws_size,
                              hipStream_t stream) {
  const u16* x = (const u16*)d_in[0];
  const u16* mem0 = (const u16*)d_in[1];
  const u16* Wq = (const u16*)d_in[2];
  const u16* Wk = (const u16*)d_in[3];
  const u16* Wv = (const u16*)d_in[4];
  const u16* Wo = (const u16*)d_in[5];
  const u16* w1 = (const u16*)d_in[8];
  const u16* b1 = (const u16*)d_in[9];
  const u16* w2 = (const u16*)d_in[10];
  const u16* b2 = (const u16*)d_in[11];
  unsigned char* ws = (unsigned char*)d_ws;
  unsigned char* xch = (unsigned char*)((((uintptr_t)(ws + WS_XCH)) + ((2u << 20) - 1)) & ~(uintptr_t)((2u << 20) - 1));
  hipFuncSetAttribute((const void*)proj_kernel, hipFuncAttributeMaxDynamicSharedMemorySize, PG_LDS);
  hipFuncSetAttribute((const void*)chain_kernel, hipFuncAttributeMaxDynamicSharedMemorySize, CH_LDS);
  hipMemsetAsync(xch, 0xFF, XCH_BYTES, stream);
  prologue_kernel<<<753, 256, 0, stream>>>(x, Wo, w2, ws, xch);
  proj_kernel<<<78 * 3, 512, PG_LDS, stream>>>(Wq, Wk, Wv, w1, b1, ws, 3);
  ChainArgs ca; ca.Wq = Wq; ca.Wk = Wk; ca.Wv = Wv; ca.Wo = Wo; ca.mem0 = mem0; ca.ws = ws; ca.xch = xch;
  chain_kernel<<<256, 512, CH_LDS, stream>>>(ca);
  attn_kernel<<<512, 256, 0, stream>>>(ws);
  out_kernel<<<256, 256, 0, stream>>>(x, b2, ws, (u16*)d_out);
}
```

```cpp
#include <hip/hip_runtime.h>
#include <hip/hip_bf16.h>
#include <stdint.h>

#define GAS __attribute__((address_space(1)))
#define LAS __attribute__((address_space(3)))
typedef unsigned short u16;
typedef unsigned v4u __attribute__((ext_vector_type(4)));
typedef unsigned v2u __attribute__((ext_vector_type(2)));
typedef float v4f __attribute__((ext_vector_type(4)));
typedef float v2f __attribute__((ext_vector_type(2)));
typedef short v4s __attribute__((ext_vector_type(4)));
typedef __bf16 v8bf __attribute__((ext_vector_type(8)));
typedef __bf16 v2bf __attribute__((ext_vector_type(2)));
typedef GAS unsigned gu32;
#define RLX_AGENT __ATOMIC_RELAXED, __HIP_MEMORY_SCOPE_AGENT

constexpr int S_ = 2048, DM = 640, NH = 32, DH = 192, HD = NH * DH, MM = 8, SEG = 32, NSEG = 64, FF = 1536;
constexpr unsigned SENT = 0xFFFFFFFFu;

constexpr size_t WS_CTL = 0;
constexpr size_t WS_XN = 524288;
constexpr size_t WS_Q = WS_XN + (size_t)S_ * DM * 2;
constexpr size_t WS_K = WS_Q + (size_t)S_ * HD * 2;
constexpr size_t WS_V = WS_K + (size_t)S_ * HD * 2;
constexpr size_t WS_O = WS_V + (size_t)S_ * HD * 2;
constexpr size_t WS_H1 = WS_O + (size_t)S_ * HD * 2;
constexpr size_t WS_WOF = WS_H1 + (size_t)S_ * FF * 2;
constexpr size_t WS_W2F = WS_WOF + (size_t)HD * DM * 2;
constexpr size_t WS_KM = WS_W2F + (size_t)FF * DM * 2;
constexpr size_t WS_PART = WS_KM + (size_t)NSEG * NH * 9216;
constexpr size_t WS_STAMP = WS_PART + (size_t)10 * S_ * DM * 4;
constexpr size_t WS_XCH = WS_STAMP + (5u << 20);
#define WSTAMP(i) do {} while (0)
constexpr size_t XCH_BYTES = 8u << 20;

__device__ __forceinline__ unsigned f2u(float f) { return __builtin_bit_cast(unsigned, f); }
__device__ __forceinline__ float u2f(unsigned u) { return __builtin_bit_cast(float, u); }
__device__ __forceinline__ v4f mfma32(v4u a, v4u b, v4f c) {
  return __builtin_amdgcn_mfma_f32_16x16x32_bf16(__builtin_bit_cast(v8bf, a), __builtin_bit_cast(v8bf, b), c, 0, 0, 0);
}
__device__ __forceinline__ v4f mfma16(v2u a, v2u b, v4f c) {
  return mfma32((v4u){a.x, a.y, 0u, 0u}, (v4u){b.x, b.y, 0u, 0u}, c);
}
__device__ __forceinline__ unsigned pk2(float lo, float hi) {
  v2f v = {lo, hi};
  return __builtin_bit_cast(unsigned, __builtin_convertvector(v, v2bf));
}
__device__ __forceinline__ float bfr(float x) {
  return u2f(pk2(x, 0.f) << 16);
}
__device__ __forceinline__ float bf_lo(unsigned w) { return u2f(w << 16); }
__device__ __forceinline__ float bf_hi(unsigned w) { return u2f(w & 0xffff0000u); }
__device__ __forceinline__ float bf2f(u16 h) { return u2f((unsigned)h << 16); }
__device__ __forceinline__ unsigned nosent(unsigned v) { return v < 0xFFFFFFFEu ? v : 0xFFFFFFFEu; }
__device__ __forceinline__ unsigned umax3(unsigned a, unsigned b, unsigned c) { return max(max(a, b), c); }

__device__ __forceinline__ v4u tr_frag(const LAS unsigned char* img, int stride, int k0, int colp, int lane) {
  const int g = lane >> 4, q = (lane & 15) >> 2;
  const LAS unsigned char* a0 = img + (k0 + 8 * g + q) * stride + colp * 2;
  v4s lo = __builtin_amdgcn_ds_read_tr16_b64_v4i16((LAS v4s*)a0);
  v4s hi = __builtin_amdgcn_ds_read_tr16_b64_v4i16((LAS v4s*)(a0 + 4 * stride));
  v2u l = __builtin_bit_cast(v2u, lo), h = __builtin_bit_cast(v2u, hi);
  return (v4u){l.x, l.y, h.x, h.y};
}

__global__ void __launch_bounds__(256) prologue_kernel(const u16* __restrict__ x, unsigned char* ws, const v4u* __restrict__ w0, const v4u* __restrict__ w1_,
                                                       const v4u* __restrict__ w2_, const v4u* __restrict__ w3) {
  unsigned char* xch = (unsigned char*)((((uintptr_t)(ws + WS_XCH)) + ((2u << 20) - 1)) & ~(uintptr_t)((2u << 20) - 1));
  const int b = blockIdx.x == 0 ? 512 : (blockIdx.x == 512 ? 0 : blockIdx.x), tid = threadIdx.x, lane = tid & 63, wave = tid >> 6;
  if (b < 512) {
    const int row = b * 4 + wave;
    if (tid < 14) { const int i = 1 + b * 7 + (tid >> 1); if (i < 3137) ((v4u*)(ws + WS_CTL))[i * 8 + (tid & 1)] = (v4u){0, 0, 0, 0}; }
    const unsigned* xr = (const unsigned*)(x + (size_t)row * DM);
    float v[10];
    float sum = 0.f;
#pragma unroll
    for (int i = 0; i < 5; ++i) { unsigned w = xr[lane + 64 * i]; v[2 * i] = bf_lo(w); v[2 * i + 1] = bf_hi(w); sum += v[2 * i] + v[2 * i + 1]; }
#pragma unroll
    for (int m = 1; m < 64; m <<= 1) sum += __shfl_xor(sum, m);
    const float mean = sum * (1.f / DM);
    float sq = 0.f;
#pragma unroll
    for (int i = 0; i < 10; ++i) { float d = v[i] - mean; sq += d * d; }
#pragma unroll
    for (int m = 1; m < 64; m <<= 1) sq += __shfl_xor(sq, m);
    const float mb = bfr(mean), vb = bfr(sq * (1.f / DM));
    const float den = bfr(sqrtf(bfr(vb + 1e-5f)));
    unsigned* xn = (unsigned*)(ws + WS_XN);
#pragma unroll
    for (int i = 0; i < 5; ++i) {
      const int n = 2 * (lane + 64 * i);
      const float a = bfr(v[2 * i] - mb) / den, c = bfr(v[2 * i + 1] - mb) / den;
      const size_t e = ((size_t)(row >> 4) * (DM / 32) + (n >> 5)) * 512 + (((n >> 3) & 3) * 16 + (row & 15)) * 8 + (n & 7);
      xn[e >> 1] = pk2(a, c);
    }
  } else if (b == 512) {
    unsigned long long best[2] = {~0ull, ~0ull};
    __amdgpu_buffer_rsrc_t r = __builtin_amdgcn_make_buffer_rsrc(xch, 0, (int)XCH_BYTES, 0x00020000);
    const unsigned base = ((unsigned)wave << 21) + (2u << 20) - 16384u + lane * 16;
    for (int rep = 0; rep < 4; ++rep) {
#pragma unroll
      for (int par = 0; par < 2; ++par) {
        unsigned long long t0, t1;
        asm volatile("s_waitcnt vmcnt(0)\n\ts_memtime %0\n\ts_waitcnt lgkmcnt(0)" : "=s"(t0) :: "memory");
        __builtin_amdgcn_raw_buffer_store_b128((v4u){1u, 2u, 3u, 4u}, r, base + par * 8192u, 0, 16);
        asm volatile("s_waitcnt vmcnt(0)\n\ts_memtime %0\n\ts_waitcnt lgkmcnt(0)" : "=s"(t1) :: "memory");
        if (t1 - t0 < best[par]) best[par] = t1 - t0;
      }
    }
    if (lane == 0) ((unsigned*)(ws + WS_CTL))[16 + wave] = best[1] < best[0] ? 1u : 0u;
    if (tid == 0) ((unsigned*)(ws + WS_CTL))[0] = 0u;
  } else if (b >= 625) {
    const int f = b - 625, m = f / 120, part = f % 120;
    const v4u* src = (m == 0 ? w0 : (m == 1 ? w1_ : (m == 2 ? w2_ : w3))) + (size_t)part * 4096 + tid;
    v4u acc = {0, 0, 0, 0};
#pragma unroll
    for (int i = 0; i < 16; ++i) acc ^= src[i * 256];
    asm volatile("" :: "v"(acc));
  } else {
    const int f = b - 513, page = f / 28, part = f % 28;
    v4u* dst = (v4u*)(xch + ((size_t)page << 21) + (size_t)part * 65536);
#pragma unroll
    for (int i = 0; i < 16; ++i) dst[i * 256 + tid] = (v4u){SENT, SENT, SENT, SENT};
  }
}

__device__ __forceinline__ float gelu_tanh(float x) {
  const float u2 = (-2.f * 0.7978845608028654f * 1.4426950408889634f) * (x + 0.044715f * x * x * x);
  return x * __builtin_amdgcn_rcpf(1.f + __builtin_amdgcn_exp2f(u2));
}
__device__ __forceinline__ float silu_f(float x) { return x * __builtin_amdgcn_rcpf(1.f + __builtin_amdgcn_exp2f(-1.4426950408889634f * x)); }

constexpr int PG_LDS = 3 * 40960;
struct MegaArgs {
  const u16 *Wq, *Wk, *Wv, *Wo, *mem0, *w1, *b1, *x, *b2, *w2;
  u16* out;
  unsigned char* ws;
  int d1, d1q, d1v, d2, d3, d4, stag, pad;
};
constexpr int CTL_KV = 1, CTL_Q = 65, CTL_H = 129, CTL_O = 193, CTL_P = 449, CTL_KM = 1089, CTL_N = 3137;
#define CNT(ctl, i) ((ctl) + (size_t)(i) * 32)
__device__ __forceinline__ void glds16(const void* g, LAS unsigned char* l) {
  unsigned keep; const unsigned la = __builtin_amdgcn_readfirstlane((unsigned)(size_t)l);
  asm volatile("s_mov_b32 %0, m0\n\ts_mov_b32 m0, %2\n\ts_nop 0\n\tglobal_load_lds_dwordx4 %1, off\n\ts_mov_b32 m0, %0" : "=&s"(keep) : "v"(g), "s"(la) : "memory");
}
__device__ __forceinline__ void glds16_sc1(const void* g, LAS unsigned char* l) {
  unsigned keep; const unsigned la = __builtin_amdgcn_readfirstlane((unsigned)(size_t)l);
  asm volatile("s_mov_b32 %0, m0\n\ts_mov_b32 m0, %2\n\ts_nop 0\n\tglobal_load_lds_dwordx4 %1, off sc1\n\ts_mov_b32 m0, %0" : "=&s"(keep) : "v"(g), "s"(la) : "memory");
}
constexpr int NPRE = 8;
__device__ __forceinline__ void prefix_role(const MegaArgs a, LAS unsigned char* lds, int w) {
  const int tid = threadIdx.x, lane = tid & 63, wave = __builtin_amdgcn_readfirstlane(tid >> 6);
  const int g = lane >> 4, q = (lane & 15) >> 2, p = lane & 3;
  const u16* W = (w < 48 ? a.Wk : a.Wv) + (w % 48) * 128;
  const unsigned char* xn = a.ws + WS_XN;
  auto stage = [&](int sg, int buf) __attribute__((always_inline)) {
#pragma unroll
    for (int i = 0; i < 5; ++i) { const int blk = wave * 5 + i;
      glds16(xn + (size_t)sg * 40960 + blk * 1024 + lane * 16, lds + buf * 40960 + blk * 1024); }
  };
  v4u wf[20];
#pragma unroll
  for (int rd = 0; rd < 2; ++rd) {
#pragma unroll
    for (int i = 0; i < 10; ++i) { const int c = (i * 8 + wave) * 64 + lane, r = c >> 4, ch = (c & 15) ^ ((r & 7) << 1);
      glds16(W + (size_t)(rd * 320 + r) * HD + ch * 8, lds + 40960 + (i * 8 + wave) * 1024); }
    if (rd == 0) stage(0, 0);
    asm volatile("s_waitcnt vmcnt(0)" ::: "memory");
    __syncthreads();
    const LAS unsigned char* b0 = lds + 40960 + (8 * g + q) * 256 + (((2 * wave + (p >> 1)) ^ (q << 1)) * 16) + (p & 1) * 8;
    const LAS unsigned char* b1 = lds + 40960 + (8 * g + q + 4) * 256 + (((2 * wave + (p >> 1)) ^ ((q + 4) << 1)) * 16) + (p & 1) * 8;
#pragma unroll
    for (int kk = 0; kk < 10; ++kk) {
      const v2u lo = __builtin_bit_cast(v2u, __builtin_amdgcn_ds_read_tr16_b64_v4i16((LAS v4s*)(b0 + kk * 8192)));
      const v2u hi = __builtin_bit_cast(v2u, __builtin_amdgcn_ds_read_tr16_b64_v4i16((LAS v4s*)(b1 + kk * 8192)));
      wf[rd * 10 + kk] = (v4u){lo.x, lo.y, hi.x, hi.y};
    }
    __syncthreads();
  }
  const __amdgpu_buffer_rsrc_t ro = __builtin_amdgcn_make_buffer_rsrc(a.ws + (w < 48 ? WS_K : WS_V), 0, S_ * HD * 2, 0x00020000);
  gu32* cnt = CNT((gu32*)(a.ws + WS_CTL), CTL_KV);
  const int n = (w % 48) * 128 + wave * 16 + 4 * g;
  const unsigned oo = (unsigned)((n >> 5) * 1024 + (((n >> 3) & 3) * 16 + (lane & 15)) * 16 + (n & 4) * 2);
  stage(1, 1);
  for (int sg = 0; sg < NPRE; ++sg) {
    if (sg + 2 < NPRE) stage(sg + 2, (sg + 2) % 3);
    v4f a0 = {0, 0, 0, 0}, a1 = a0;
    const LAS unsigned char* A = lds + (sg % 3) * 40960 + lane * 16;
#pragma unroll
    for (int kk = 0; kk < 20; ++kk) { a0 = mfma32(wf[kk], *(const LAS v4u*)(A + kk * 1024), a0); a1 = mfma32(wf[kk], *(const LAS v4u*)(A + 20480 + kk * 1024), a1); }
    __builtin_amdgcn_raw_buffer_store_b64((v2u){pk2(a0[0], a0[1]), pk2(a0[2], a0[3])}, ro, oo + (unsigned)(sg * 2) * 196608u, 0, 16);
    __builtin_amdgcn_raw_buffer_store_b64((v2u){pk2(a1[0], a1[1]), pk2(a1[2], a1[3])}, ro, oo + (unsigned)(sg * 2 + 1) * 196608u, 0, 16);
    asm volatile("s_waitcnt vmcnt(0)" ::: "memory");
    __syncthreads();
    if (tid == 0) __hip_atomic_fetch_add(CNT(cnt, sg), 1u, RLX_AGENT);
  }
}
__device__ __forceinline__ void proj_role(const MegaArgs a, LAS unsigned char* lds, int grp, int mode) {
  const int nit = mode < 2 ? NSEG - NPRE : NSEG;
  const unsigned inc = mode < 2 ? 2u : 1u;
  auto seg = [&](int it) __attribute__((always_inline)) -> int { return mode < 2 ? NPRE + it : it; };
  const int tid = threadIdx.x, lane = tid & 63, wave = __builtin_amdgcn_readfirstlane(tid >> 6);
  unsigned char* ws = a.ws;
  const u16* W; int ldw, n0; size_t outo; int ncb; bool ffn = false;
  if (grp < 72) { const int m = grp / 24; W = m == 0 ? a.Wk : (m == 1 ? a.Wv : a.Wq); ldw = HD; n0 = (grp % 24) * 256;
                  outo = (m == 0 ? WS_K : (m == 1 ? WS_V : WS_Q)); ncb = HD / 32; }
  else { W = a.w1; ldw = FF; n0 = (grp - 72) * 256; outo = WS_H1; ncb = FF / 32; ffn = true; }
  v4u wf[20][2];
#pragma unroll
  for (int rd = 0; rd < 3; ++rd) {
    const int nk = rd < 2 ? 7 : 6;
#pragma unroll
    for (int i = 0; i < 2 * nk; ++i) { const int c = (i * 8 + wave) * 64 + lane, r = c >> 5, ch = (c & 31) ^ ((r & 3) << 2);
      glds16(W + (size_t)(rd * 224 + r) * ldw + n0 + ch * 8, lds + (i * 8 + wave) * 1024); }
    asm volatile("s_waitcnt vmcnt(0)" ::: "memory");
    __syncthreads();
    const int g = lane >> 4, q = (lane & 15) >> 2, p = lane & 3;
    const LAS unsigned char* b0 = lds + (8 * g + q) * 512 + (((wave * 4 + p) ^ (q << 2)) * 16);
#pragma unroll
    for (int kk = 0; kk < nk; ++kk)
#pragma unroll
      for (int t = 0; t < 2; ++t) {
        const v2u lo = __builtin_bit_cast(v2u, __builtin_amdgcn_ds_read_tr16_b64_v4i16((LAS v4s*)(b0 + kk * 16384 + t * 8)));
        const v2u hi = __builtin_bit_cast(v2u, __builtin_amdgcn_ds_read_tr16_b64_v4i16((LAS v4s*)(b0 + kk * 16384 + t * 8 + 2048)));
        wf[rd * 7 + kk][t] = (v4u){lo.x, lo.y, hi.x, hi.y};
      }
    __syncthreads();
  }
  float bias[8];
#pragma unroll
  for (int i = 0; i < 8; ++i) bias[i] = ffn ? bf2f(a.b1[n0 + wave * 32 + 8 * (lane >> 4) + i]) : 0.f;
  const unsigned char* xn = ws + WS_XN;
  const __amdgpu_buffer_rsrc_t ro = __builtin_amdgcn_make_buffer_rsrc(ws + outo, 0, S_ * HD * 2, 0x00020000);
  gu32* cnt = CNT((gu32*)(ws + WS_CTL), grp < 48 ? CTL_KV : (grp < 72 ? CTL_Q : CTL_H));
  auto stage = [&](int s, int buf) __attribute__((always_inline)) {
#pragma unroll
    for (int i = 0; i < 5; ++i) { const int blk = wave * 5 + i;
      glds16(xn + (size_t)s * 40960 + blk * 1024 + lane * 16, lds + buf * 40960 + blk * 1024); }
  };
  WSTAMP(3);
  stage(seg(0), 0);
  stage(seg(1), 1);
  int buf = 0;
  asm volatile("s_waitcnt vmcnt(5)" ::: "memory");
  for (int it = 0; it < nit; ++it) {
    const int s = seg(it);
    __builtin_amdgcn_s_barrier();
    asm volatile("" ::: "memory");
    if (it >= 5 && tid == 0) __hip_atomic_fetch_add(CNT(cnt, seg(it - 2)), inc, RLX_AGENT);
    int nb = buf + 2; if (nb >= 3) nb -= 3;
    const bool more = it + 2 < nit;
    if (more) stage(seg(it + 2), nb);
    v4f acc[2][2];
#pragma unroll
    for (int x = 0; x < 2; ++x) { acc[x][0] = (v4f){0, 0, 0, 0}; acc[x][1] = (v4f){0, 0, 0, 0}; }
    const LAS unsigned char* A = lds + buf * 40960 + lane * 16;
    v4u x0[3], x1[3];
#pragma unroll
    for (int kk = 0; kk < 2; ++kk) { x0[kk] = *(const LAS v4u*)(A + kk * 1024); x1[kk] = *(const LAS v4u*)(A + 20480 + kk * 1024); }
    __builtin_amdgcn_sched_group_barrier(0x100, 4, 0);
#pragma unroll
    for (int kk = 0; kk < 20; ++kk) {
      if (kk + 2 < 20) { x0[(kk + 2) % 3] = *(const LAS v4u*)(A + (kk + 2) * 1024); x1[(kk + 2) % 3] = *(const LAS v4u*)(A + 20480 + (kk + 2) * 1024); }
      acc[0][0] = mfma32(wf[kk][0], x0[kk % 3], acc[0][0]); acc[0][1] = mfma32(wf[kk][1], x0[kk % 3], acc[0][1]);
      acc[1][0] = mfma32(wf[kk][0], x1[kk % 3], acc[1][0]); acc[1][1] = mfma32(wf[kk][1], x1[kk % 3], acc[1][1]);
      __builtin_amdgcn_sched_group_barrier(0x100, 2, 0);
      __builtin_amdgcn_sched_group_barrier(0x8, 4, 0);
    }
#pragma unroll
    for (int x = 0; x < 2; ++x) {
      float o[8];
#pragma unroll
      for (int r = 0; r < 4; ++r) { o[r] = acc[x][0][r]; o[4 + r] = acc[x][1][r]; }
      if (ffn) {
#pragma unroll
        for (int i = 0; i < 8; ++i) o[i] = gelu_tanh(bfr(bfr(o[i]) + bias[i]));
      }
      v4u pkd = {pk2(o[0], o[1]), pk2(o[2], o[3]), pk2(o[4], o[5]), pk2(o[6], o[7])};
      __builtin_amdgcn_raw_buffer_store_b128(pkd, ro, (unsigned)(((s * 2 + x) * ncb + (n0 >> 5) + wave) * 1024 + lane * 16), 0, 16);
    }
    if (it < 3) {
      asm volatile("s_waitcnt vmcnt(0)" ::: "memory");
      __builtin_amdgcn_s_barrier();
      if (tid == 0) __hip_atomic_fetch_add(CNT(cnt, s), inc, RLX_AGENT);
    }
    if (more) asm volatile("s_waitcnt vmcnt(7)" ::: "memory"); else asm volatile("s_waitcnt vmcnt(2)" ::: "memory");
    buf = buf + 1; if (buf >= 3) buf = 0;
  }
  asm volatile("s_waitcnt vmcnt(0)" ::: "memory");
  __builtin_amdgcn_s_barrier();
  if (tid == 0) {
    __hip_atomic_fetch_add(CNT(cnt, seg(nit - 2)), inc, RLX_AGENT);
    __hip_atomic_fetch_add(CNT(cnt, seg(nit - 1)), inc, RLX_AGENT);
  }
}

__device__ __forceinline__ void wait_cnt(gu32* ctl, int idx, unsigned need) {
  for (unsigned sp = 0; sp < 200000u; ++sp) {
    if (__builtin_amdgcn_readfirstlane(__hip_atomic_load(CNT(ctl, idx), RLX_AGENT)) >= need) break;
    if ((sp & 63u) == 63u && __builtin_amdgcn_readfirstlane(__hip_atomic_load(ctl, RLX_AGENT)) != 0u) break;
    __builtin_amdgcn_s_sleep(8);
  }
  asm volatile("" ::: "memory");
}

constexpr int AT_VSTRIDE = 400;
__device__ __forceinline__ void att_role(const MegaArgs a, LAS unsigned char* lds, int aw) {
  const int tid = threadIdx.x, lane = tid & 63, wave = __builtin_amdgcn_readfirstlane(tid >> 6);
  const int g = lane >> 4, r16 = lane & 15;
  const int h = (aw & 3) * 8 + wave;
  LAS unsigned char* vimg = lds + wave * 40 * AT_VSTRIDE;
  gu32* ctl = (gu32*)(a.ws + WS_CTL);
  const __amdgpu_buffer_rsrc_t rq = __builtin_amdgcn_make_buffer_rsrc(a.ws + WS_Q, 0, 4 * S_ * HD * 2, 0x00020000);
  const __amdgpu_buffer_rsrc_t rk = __builtin_amdgcn_make_buffer_rsrc(a.ws + WS_KM, 0, NSEG * NH * 9216, 0x00020000);
  constexpr unsigned MAT = S_ * HD * 2;
  const float cexp = 0.07216878364870322f * 1.4426950408889634f;
  const int q4 = (lane & 15) >> 2, p4 = lane & 3;
  for (int s = aw >> 2; s < NSEG; s += 4) {
    const unsigned fo = (unsigned)((s * 2 * (HD / 32) + h * 6) * 1024 + lane * 16);
    wait_cnt(ctl, CTL_Q + s, 24u);
    wait_cnt(ctl, CTL_KV + s, 96u);
    v4f t[2][3];
#pragma unroll
    for (int qt = 0; qt < 2; ++qt) { t[qt][0] = (v4f){0, 0, 0, 0}; t[qt][1] = t[qt][0]; t[qt][2] = t[qt][0]; }
    v4u q[2][6];
    {
      v4u v[12];
#pragma unroll
      for (int i = 0; i < 12; ++i) v[i] = __builtin_amdgcn_raw_buffer_load_b128(rq, fo + 2 * MAT + (i / 6) * 196608u + (i % 6) * 1024u, 0, 16);
#pragma unroll
      for (int i = 0; i < 12; ++i) q[i / 6][i % 6] = __builtin_amdgcn_raw_buffer_load_b128(rq, fo + (i / 6) * 196608u + (i % 6) * 1024u, 0, 16);
#pragma unroll
      for (int i = 0; i < 12; ++i) *(LAS v4u*)(vimg + (8 + (i / 6) * 16 + r16) * AT_VSTRIDE + (32 * (i % 6) + 8 * g) * 2) = v[i];
#pragma unroll
      for (int i = 0; i < 12; ++i) v[i] = __builtin_amdgcn_raw_buffer_load_b128(rq, fo + MAT + (i / 6) * 196608u + (i % 6) * 1024u, 0, 16);
#pragma unroll
      for (int kk = 0; kk < 6; ++kk)
#pragma unroll
        for (int qt = 0; qt < 2; ++qt) { t[qt][1] = mfma32(v[kk], q[qt][kk], t[qt][1]); if (qt == 1) t[qt][2] = mfma32(v[6 + kk], q[qt][kk], t[qt][2]); }
    }
    WSTAMP(8 + (s >> 2) * 4);
    wait_cnt(ctl, CTL_KM + s * NH + h, 4u);
    WSTAMP(9 + (s >> 2) * 4);
    {
      const unsigned ko = (unsigned)((s * NH + h) * 9216);
      v4u km[6], vm[3];
#pragma unroll
      for (int kk = 0; kk < 6; ++kk) km[kk] = __builtin_amdgcn_raw_buffer_load_b128(rk, ko + (3 + (kk >> 1)) * 1024 + (g * 16 + (kk & 1) * 8 + (r16 & 7)) * 16, 0, 16);
#pragma unroll
      for (int pp = 0; pp < 3; ++pp) vm[pp] = __builtin_amdgcn_raw_buffer_load_b128(rk, ko + (6 + pp) * 1024 + lane * 16, 0, 16);
#pragma unroll
      for (int kk = 0; kk < 6; ++kk)
#pragma unroll
        for (int qt = 0; qt < 2; ++qt) t[qt][0] = mfma32(km[kk], q[qt][kk], t[qt][0]);
#pragma unroll
      for (int pp = 0; pp < 3; ++pp) *(LAS v4u*)(vimg + (r16 & 7) * AT_VSTRIDE + (64 * pp + 32 * (r16 >> 3) + 8 * g) * 2) = vm[pp];
    }
    v2u pm[2]; v4u px[2];
#pragma unroll
    for (int qt = 0; qt < 2; ++qt) {
      const int qi = qt * 16 + r16;
      float sc[12];
#pragma unroll
      for (int r = 0; r < 4; ++r) {
        sc[r] = g < 2 ? t[qt][0][r] : -INFINITY;
        sc[4 + r] = (4 * g + r <= qi) ? t[qt][1][r] : -INFINITY;
        sc[8 + r] = (16 + 4 * g + r <= qi) ? t[qt][2][r] : -INFINITY;
      }
      float mx = sc[0];
#pragma unroll
      for (int i = 1; i < 12; ++i) mx = fmaxf(mx, sc[i]);
      mx = fmaxf(mx, __shfl_xor(mx, 16)); mx = fmaxf(mx, __shfl_xor(mx, 32));
      const float mc = mx * cexp;
      float p[12], l = 0.f;
#pragma unroll
      for (int i = 0; i < 12; ++i) { p[i] = __builtin_amdgcn_exp2f(sc[i] * cexp - mc); l += p[i]; }
      l += __shfl_xor(l, 16); l += __shfl_xor(l, 32);
      const float li = 1.f / l;
#pragma unroll
      for (int i = 0; i < 12; ++i) p[i] *= li;
      pm[qt] = (v2u){pk2(p[0], p[1]), pk2(p[2], p[3])};
      px[qt] = (v4u){pk2(p[4], p[5]), pk2(p[6], p[7]), pk2(p[8], p[9]), pk2(p[10], p[11])};
    }
    asm volatile("s_waitcnt lgkmcnt(0)" ::: "memory");
#pragma unroll
    for (int kk = 0; kk < 6; ++kk) {
      v4f o[2][2];
#pragma unroll
      for (int tt = 0; tt < 2; ++tt) {
        const int col = 32 * kk + 8 * p4 + 4 * tt;
        const LAS unsigned char* bm = vimg + (4 * (g & 1) + q4) * AT_VSTRIDE + col * 2;
        const LAS unsigned char* bx = vimg + (8 + 4 * g + q4) * AT_VSTRIDE + col * 2;
        const v2u vm = __builtin_bit_cast(v2u, __builtin_amdgcn_ds_read_tr16_b64_v4i16((LAS v4s*)bm));
        const v2u v0 = __builtin_bit_cast(v2u, __builtin_amdgcn_ds_read_tr16_b64_v4i16((LAS v4s*)bx));
        const v2u v1 = __builtin_bit_cast(v2u, __builtin_amdgcn_ds_read_tr16_b64_v4i16((LAS v4s*)(bx + 16 * AT_VSTRIDE)));
        const v4u vx = {v0.x, v0.y, v1.x, v1.y};
#pragma unroll
        for (int qt = 0; qt < 2; ++qt) { o[qt][tt] = mfma32(vx, px[qt], (v4f){0, 0, 0, 0}); o[qt][tt] = mfma16(vm, pm[qt], o[qt][tt]); }
      }
#pragma unroll
      for (int qt = 0; qt < 2; ++qt) {
        v4u pkd = {pk2(o[qt][0][0], o[qt][0][1]), pk2(o[qt][0][2], o[qt][0][3]), pk2(o[qt][1][0], o[qt][1][1]), pk2(o[qt][1][2], o[qt][1][3])};
        __builtin_amdgcn_raw_buffer_store_b128(pkd, rq, fo + 3 * MAT + qt * 196608u + kk * 1024u, 0, 16);
      }
    }
    asm volatile("s_waitcnt vmcnt(0) lgkmcnt(0)" ::: "memory");
    if (lane == 0) __hip_atomic_fetch_add(CNT(ctl, CTL_O + s * 4 + (h >> 3)), 1u, RLX_AGENT);
    WSTAMP(10 + (s >> 2) * 4);
  }
}

constexpr int OSPLIT = 56;
__device__ __forceinline__ void out_role(const MegaArgs a, LAS unsigned char* lds, int kg, int ng, int set) {
  const int NT = set == 0 ? OSPLIT + (128 - OSPLIT) / 3 : (128 - OSPLIT) / 3;
  auto tile = [&](int i) __attribute__((always_inline)) -> int { return set == 0 ? (i < OSPLIT ? i : OSPLIT + 3 * (i - OSPLIT)) : OSPLIT + set + 3 * i; };
  const int tid = threadIdx.x, lane = tid & 63, wave = __builtin_amdgcn_readfirstlane(tid >> 6);
  gu32* ctl = (gu32*)(a.ws + WS_CTL);
  const int cp = wave & 3, kh = wave >> 2;
  v4u wf[24][2];
  {
    const u16* W = (kg < 4 ? a.Wo + (size_t)kg * 1536 * DM : a.w2) + ng * 128;
    const int g = lane >> 4, q = (lane & 15) >> 2, p = lane & 3;
#pragma unroll
    for (int rd = 0; rd < 3; ++rd) {
#pragma unroll 4
      for (int i = 0; i < 16; ++i) { const int c = (i * 8 + wave) * 64 + lane, r = c >> 4, ch = (c & 15) ^ ((r & 7) << 1);
        glds16(W + (size_t)((r >> 8) * 768 + rd * 256 + (r & 255)) * DM + ch * 8, lds + (i * 8 + wave) * 1024); }
      asm volatile("s_waitcnt vmcnt(0)" ::: "memory");
      __syncthreads();
#pragma unroll
      for (int t = 0; t < 2; ++t) {
        const LAS unsigned char* b0 = lds + kh * 65536 + (8 * g + q) * 256 + (((4 * cp + 2 * t + (p >> 1)) ^ (q << 1)) * 16) + (p & 1) * 8;
        const LAS unsigned char* b1 = lds + kh * 65536 + (8 * g + q + 4) * 256 + (((4 * cp + 2 * t + (p >> 1)) ^ ((q + 4) << 1)) * 16) + (p & 1) * 8;
#pragma unroll
        for (int kk = 0; kk < 8; ++kk) {
          const v2u lo = __builtin_bit_cast(v2u, __builtin_amdgcn_ds_read_tr16_b64_v4i16((LAS v4s*)(b0 + kk * 8192)));
          const v2u hi = __builtin_bit_cast(v2u, __builtin_amdgcn_ds_read_tr16_b64_v4i16((LAS v4s*)(b1 + kk * 8192)));
          wf[rd * 8 + kk][t] = (v4u){lo.x, lo.y, hi.x, hi.y};
        }
      }
      __syncthreads();
    }
  }
  const unsigned char* asrc = (kg < 4 ? a.ws + WS_O + (size_t)kg * 49152 : a.ws + WS_H1) + wave * 6144 + lane * 16;
  const size_t astride = kg < 4 ? 196608 : 49152;
  const __amdgpu_buffer_rsrc_t rp = __builtin_amdgcn_make_buffer_rsrc(a.ws + WS_PART, 0, 10 * S_ * DM * 4, 0x00020000);
  const int cbase = kg < 4 ? CTL_O + kg : CTL_H, cstep = kg < 4 ? 4 : 1;
  const unsigned need = kg < 4 ? 8u : 6u;
  auto stage = [&](int i) __attribute__((always_inline)) {
    const int buf = i % 3;
#pragma unroll
    for (int k = 0; k < 6; ++k) glds16_sc1(asrc + (size_t)tile(i) * astride + k * 1024, lds + buf * 49152 + (wave * 6 + k) * 1024);
  };
  auto signal = [&](int i) __attribute__((always_inline)) {
    if (lane == 0) __hip_atomic_fetch_add(CNT(ctl, CTL_P + tile(i) * 5 + ng) + cp, 1u, RLX_AGENT); };
  WSTAMP(4);
  int staged = 0, sig = 0;
  for (int i = 0; i < NT; ++i) {
    if (staged <= i) {
      asm volatile("s_waitcnt vmcnt(0)" ::: "memory");
      for (; sig < i; ++sig) signal(sig);
      wait_cnt(ctl, cbase + (tile(i) >> 1) * cstep, need);
      stage(i); staged = i + 1;
      asm volatile("s_waitcnt vmcnt(0)" ::: "memory");
    }
    __builtin_amdgcn_s_barrier();
    asm volatile("" ::: "memory");
    WSTAMP(8 + i);
    const int n0 = staged < NT ? staged : NT - 1, n1 = staged + 1 < NT ? staged + 1 : NT - 1;
    const unsigned c0 = __hip_atomic_load(CNT(ctl, cbase + (tile(n0) >> 1) * cstep), RLX_AGENT);
    const unsigned c1 = __hip_atomic_load(CNT(ctl, cbase + (tile(n1) >> 1) * cstep), RLX_AGENT);
    v4f acc0 = {0, 0, 0, 0}, acc1 = acc0;
    const LAS unsigned char* A = lds + (i % 3) * 49152 + kh * 24576 + lane * 16;
    v4u x[4];
#pragma unroll
    for (int k = 0; k < 3; ++k) x[k] = *(const LAS v4u*)(A + k * 1024);
    __builtin_amdgcn_sched_group_barrier(0x100, 3, 0);
#pragma unroll
    for (int k = 0; k < 24; ++k) {
      if (k + 3 < 24) x[(k + 3) & 3] = *(const LAS v4u*)(A + (k + 3) * 1024);
      acc0 = mfma32(wf[k][0], x[k & 3], acc0); acc1 = mfma32(wf[k][1], x[k & 3], acc1);
      __builtin_amdgcn_sched_group_barrier(0x100, 1, 0);
      __builtin_amdgcn_sched_group_barrier(0x8, 2, 0);
    }
    asm volatile("s_waitcnt vmcnt(0)" ::: "memory");
    const bool r0 = __builtin_amdgcn_readfirstlane(c0) >= need, r1 = __builtin_amdgcn_readfirstlane(c1) >= need;
    for (; sig < i; ++sig) signal(sig);
    const unsigned po = (unsigned)(((kg * 2 + kh) * 128 + tile(i)) * 40 + ng * 8 + 2 * cp) * 1024u + lane * 16u;
    __builtin_amdgcn_raw_buffer_store_b128(__builtin_bit_cast(v4u, acc0), rp, po, 0, 16);
    __builtin_amdgcn_raw_buffer_store_b128(__builtin_bit_cast(v4u, acc1), rp, po + 1024u, 0, 16);
    bool late = false;
    if (staged < NT && staged <= i + 2 && r0) { late = staged == i + 1; stage(staged); ++staged;
      if (staged < NT && staged <= i + 2 && r1) { stage(staged); ++staged; if (late) asm volatile("s_waitcnt vmcnt(6)" ::: "memory"); late = false; } }
    if (late) asm volatile("s_waitcnt vmcnt(0)" ::: "memory");
  }
  asm volatile("s_waitcnt vmcnt(0)" ::: "memory");
  for (; sig < NT; ++sig) signal(sig);
}

__device__ __forceinline__ void fin_role(const MegaArgs a, int fw, int nfw) {
  const int tid = threadIdx.x, lane = tid & 63, wave = __builtin_amdgcn_readfirstlane(tid >> 6);
  const int g = lane >> 4, r16 = lane & 15;
  gu32* ctl = (gu32*)(a.ws + WS_CTL);
  const __amdgpu_buffer_rsrc_t rp = __builtin_amdgcn_make_buffer_rsrc(a.ws + WS_PART, 0, 10 * S_ * DM * 4, 0x00020000);
  for (int u = fw * 8 + wave; u < 1280; u += nfw * 8) {
    const int rt = u / 10, hg = u % 10;
    const int row = rt * 16 + r16;
    v2u xv[4], bv[4];
#pragma unroll
    for (int d = 0; d < 4; ++d) { const int col = (hg * 4 + d) * 16 + 4 * g;
      xv[d] = *(const v2u*)(a.x + (size_t)row * DM + col); bv[d] = *(const v2u*)(a.b2 + col); }
    for (unsigned sp = 0; sp < 400000u; ++sp) {
      const unsigned c = __hip_atomic_load(CNT(ctl, CTL_P + rt * 5 + (hg >> 1)) + (hg & 1) * 2 + (lane & 1), RLX_AGENT);
      if (__builtin_amdgcn_ballot_w64(c < 10u) == 0ull) break;
      if ((sp & 63u) == 63u && __builtin_amdgcn_readfirstlane(__hip_atomic_load(ctl, RLX_AGENT)) != 0u) break;
      __builtin_amdgcn_s_sleep(4);
    }
    asm volatile("" ::: "memory");
    v4f pp[4][10];
#pragma unroll
    for (int d = 0; d < 4; ++d)
#pragma unroll
      for (int k = 0; k < 10; ++k)
        pp[d][k] = __builtin_bit_cast(v4f, __builtin_amdgcn_raw_buffer_load_b128(rp, (unsigned)((k * 128 + rt) * 40 + hg * 4 + d) * 1024u + lane * 16u, 0, 16));
#pragma unroll
    for (int d = 0; d < 4; ++d) {
      const v4f sa = ((pp[d][0] + pp[d][1]) + (pp[d][2] + pp[d][3])) + ((pp[d][4] + pp[d][5]) + (pp[d][6] + pp[d][7]));
      const v4f sg = pp[d][8] + pp[d][9];
      const float xs[4] = {bf_lo(xv[d].x), bf_hi(xv[d].x), bf_lo(xv[d].y), bf_hi(xv[d].y)};
      const float bs[4] = {bf_lo(bv[d].x), bf_hi(bv[d].x), bf_lo(bv[d].y), bf_hi(bv[d].y)};
      float o[4];
#pragma unroll
      for (int r = 0; r < 4; ++r) {
        const float hh = bfr(bfr(sg[r]) + bs[r]);
        o[r] = xs[r] + bfr(bfr(sa[r]) * bfr(silu_f(hh)));
      }
      *(v2u*)(a.out + (size_t)row * DM + (hg * 4 + d) * 16 + 4 * g) = (v2u){pk2(o[0], o[1]), pk2(o[2], o[3])};
    }
  }
}

#define DELAY(n) do { for (int i_ = 0; i_ < (n); ++i_) __builtin_amdgcn_s_sleep(1); } while (0)
__device__ __forceinline__ float fmax3(float a, float b, float c) { float r; asm("v_max3_f32 %0, %1, %2, %3" : "=v"(r) : "v"(a), "v"(b), "v"(c)); return r; }
constexpr int CL_MEM = 0, CL_VX = 10240, CL_KX = CL_VX + 24576, CL_PB = CL_KX + 12288, CL_P9 = CL_PB + 3072, CL_UY = CL_P9 + 3072, CL_FLAG = CL_UY + 4096;
constexpr int CH_LDS = 3 * 49152;
constexpr unsigned SPIN_LIMIT = 100000u;
#define STAMP(i) do {} while (0)
#define STAMPL(i) do {} while (0)
#define CST(i) do {} while (0)
#define STAMPV(i, v) do {} while (0)
#define STAMP_DECL do {} while (0)
#define STAMP_FLUSH(s) do {} while (0)

typedef unsigned v8u __attribute__((ext_vector_type(8)));
typedef __bf16 v16bf __attribute__((ext_vector_type(16)));
__device__ __forceinline__ v4u ldx(__amdgpu_buffer_rsrc_t r, unsigned voff, unsigned soff) { return __builtin_amdgcn_raw_buffer_load_b128(r, voff, soff, 16); }
__device__ __forceinline__ void stx16(__amdgpu_buffer_rsrc_t r, unsigned voff, v4u v) { __builtin_amdgcn_raw_buffer_store_b128(v, r, voff, 0, 16); }
__device__ __forceinline__ void stx4(__amdgpu_buffer_rsrc_t r, unsigned voff, unsigned soff, unsigned v) { __builtin_amdgcn_raw_buffer_store_b32(v, r, voff, soff, 16); }
__device__ __forceinline__ unsigned xmap(unsigned o, unsigned par) { return ((o >> 13) << 14) + (par << 13) + (o & 8191u); }
__device__ __forceinline__ float dpp_shl8(float v) {
  return u2f((unsigned)__builtin_amdgcn_update_dpp(0, (int)f2u(v), 0x108, 0xf, 0xf, true));
}
__device__ __forceinline__ float dpp_xor1(float v) {
  return u2f((unsigned)__builtin_amdgcn_update_dpp(0, (int)f2u(v), 0xB1, 0xf, 0xf, true));
}
__device__ __forceinline__ float swap32_add(float a, float b) {
  auto r = __builtin_amdgcn_permlane32_swap(f2u(a), f2u(b), false, false); return u2f(r[0]) + u2f(r[1]);
}
__device__ __forceinline__ float swap16_add(float a) { auto r = __builtin_amdgcn_permlane16_swap(f2u(a), f2u(a), false, false); return u2f(r[0]) + u2f(r[1]); }
__device__ __forceinline__ float fmax2(float a, float b) { float r; asm("v_max_f32 %0, %1, %2" : "=v"(r) : "v"(a), "v"(b)); return r; }
__device__ __forceinline__ float swap16_max(float a) { auto r = __builtin_amdgcn_permlane16_swap(f2u(a), f2u(a), false, false); return fmax2(u2f(r[0]), u2f(r[1])); }
__device__ __forceinline__ float swap32_max(float a) { auto r = __builtin_amdgcn_permlane32_swap(f2u(a), f2u(a), false, false); return fmax2(u2f(r[0]), u2f(r[1])); }
__device__ __forceinline__ v4f smfma(v4u a, v8u b, v4f c, int idx) {
  return __builtin_amdgcn_smfmac_f32_16x16x64_bf16(__builtin_bit_cast(v8bf, a), __builtin_bit_cast(v16bf, b), c, idx, 0, 0);
}
__device__ __forceinline__ v4f mfma16z(v2u a, v2u b) {
  return __builtin_amdgcn_mfma_f32_16x16x16bf16_1k(__builtin_bit_cast(v4s, a), __builtin_bit_cast(v4s, b), (v4f){0, 0, 0, 0}, 0, 0, 0);
}
__device__ __forceinline__ v8u tr_frag64(const LAS unsigned char* img, int stride, int k0, int colp, int lane) {
  const int g = lane >> 4, q = (lane & 15) >> 2;
  const LAS unsigned char* a0 = img + (k0 + 4 * g + q) * stride + colp * 2;
  const v2u t0 = __builtin_bit_cast(v2u, __builtin_amdgcn_ds_read_tr16_b64_v4i16((LAS v4s*)a0));
  const v2u t32 = __builtin_bit_cast(v2u, __builtin_amdgcn_ds_read_tr16_b64_v4i16((LAS v4s*)(a0 + 32 * stride)));
  const v2u t16 = __builtin_bit_cast(v2u, __builtin_amdgcn_ds_read_tr16_b64_v4i16((LAS v4s*)(a0 + 16 * stride)));
  const v2u t48 = __builtin_bit_cast(v2u, __builtin_amdgcn_ds_read_tr16_b64_v4i16((LAS v4s*)(a0 + 48 * stride)));
  return (v8u){t0.x, t32.x, t0.y, t32.y, t16.x, t48.x, t16.y, t48.y};
}
typedef unsigned v16u __attribute__((ext_vector_type(16)));
typedef unsigned v32u __attribute__((ext_vector_type(32)));
__device__ __forceinline__ void tr_mem2(unsigned v0, unsigned la, unsigned& t, unsigned& cnt, __amdgpu_buffer_rsrc_t rs, unsigned q) {
  asm volatile(
    "1:\n\t"
    "s_memtime s[98:99]\n\t"
    "s_waitcnt lgkmcnt(0)\n\t"
    "s_sub_u32 s99, %0, s98\n\t"
    "s_sub_u32 s99, s99, 24\n\t"
    "s_cmp_gt_u32 s99, 0x100000\n\t"
    "s_cbranch_scc1 5f\n\t"
    "s_cmp_lt_u32 s99, 96\n\t"
    "s_cbranch_scc1 1b\n\t"
    "s_sleep 1\n\t"
    "s_branch 1b\n\t"
    "5:\n\t"
    "buffer_load_dwordx4 v[244:247], %2, %4, 0 offen offset:0 sc1\n\t"
    "buffer_load_dwordx4 v[248:251], %2, %4, 0 offen offset:1024 sc1\n\t"
    "s_waitcnt vmcnt(1)\n\t"
    "v_max3_u32 v243, v244, v245, v246\n\t"
    "v_max_u32 v243, v243, v247\n\t"
    "ds_write_b128 %3, v[244:247] offset:0\n\t"
    "s_waitcnt vmcnt(0)\n\t"
    "v_max3_u32 v243, v243, v248, v249\n\t"
    "v_max3_u32 v243, v243, v250, v251\n\t"
    "v_cmp_eq_u32 vcc, -1, v243\n\t"
    "ds_write_b128 %3, v[248:251] offset:1024\n\t"
    "s_cbranch_vccz 3f\n\t"
    "s_sub_u32 %1, %1, 1\n\t"
    "s_cmp_eq_u32 %1, 0\n\t"
    "s_cbranch_scc1 3f\n\t"
    "s_cmp_eq_u32 %5, 0\n\t"
    "s_cbranch_scc1 5b\n\t"
    "s_memtime s[98:99]\n\t"
    "s_waitcnt lgkmcnt(0)\n\t"
    "2:\n\t"
    "s_add_u32 %0, %0, %5\n\t"
    "s_sub_u32 s99, s98, %0\n\t"
    "s_cmp_gt_i32 s99, 0\n\t"
    "s_cbranch_scc1 2b\n\t"
    "s_branch 1b\n\t"
    "3:\n\t"
    "s_waitcnt lgkmcnt(0)\n\t"
    : "+s"(t), "+s"(cnt)
    : "v"(v0), "v"(la), "s"(rs), "s"(q)
    : "v244", "v245", "v246", "v247", "v248", "v249", "v250", "v251", "v243", "s98", "s99", "vcc", "scc", "memory");
}
__device__ __forceinline__ void tr_mem3(unsigned v0, unsigned la, unsigned& t, unsigned& cnt, __amdgpu_buffer_rsrc_t rs, unsigned q) {
  asm volatile(
    "1:\n\t"
    "s_memtime s[98:99]\n\t"
    "s_waitcnt lgkmcnt(0)\n\t"
    "s_sub_u32 s99, %0, s98\n\t"
    "s_sub_u32 s99, s99, 24\n\t"
    "s_cmp_gt_u32 s99, 0x100000\n\t"
    "s_cbranch_scc1 5f\n\t"
    "s_cmp_lt_u32 s99, 96\n\t"
    "s_cbranch_scc1 1b\n\t"
    "s_sleep 1\n\t"
    "s_branch 1b\n\t"
    "5:\n\t"
    "buffer_load_dwordx4 v[244:247], %2, %4, 0 offen offset:0 sc1\n\t"
    "buffer_load_dwordx4 v[248:251], %2, %4, 0 offen offset:1024 sc1\n\t"
    "buffer_load_dwordx4 v[252:255], %2, %4, 0 offen offset:2048 sc1\n\t"
    "s_waitcnt vmcnt(2)\n\t"
    "v_max3_u32 v243, v244, v245, v246\n\t"
    "v_max_u32 v243, v243, v247\n\t"
    "ds_write_b128 %3, v[244:247] offset:0\n\t"
    "s_waitcnt vmcnt(1)\n\t"
    "v_max3_u32 v243, v243, v248, v249\n\t"
    "v_max3_u32 v243, v243, v250, v251\n\t"
    "ds_write_b128 %3, v[248:251] offset:1024\n\t"
    "s_waitcnt vmcnt(0)\n\t"
    "v_max3_u32 v243, v243, v252, v253\n\t"
    "v_max3_u32 v243, v243, v254, v255\n\t"
    "v_cmp_eq_u32 vcc, -1, v243\n\t"
    "ds_write_b128 %3, v[252:255] offset:2048\n\t"
    "s_cbranch_vccz 3f\n\t"
    "s_sub_u32 %1, %1, 1\n\t"
    "s_cmp_eq_u32 %1, 0\n\t"
    "s_cbranch_scc1 3f\n\t"
    "s_cmp_eq_u32 %5, 0\n\t"
    "s_cbranch_scc1 5b\n\t"
    "s_memtime s[98:99]\n\t"
    "s_waitcnt lgkmcnt(0)\n\t"
    "2:\n\t"
    "s_add_u32 %0, %0, %5\n\t"
    "s_sub_u32 s99, s98, %0\n\t"
    "s_cmp_gt_i32 s99, 0\n\t"
    "s_cbranch_scc1 2b\n\t"
    "s_branch 1b\n\t"
    "3:\n\t"
    "s_waitcnt lgkmcnt(0)\n\t"
    : "+s"(t), "+s"(cnt)
    : "v"(v0), "v"(la), "s"(rs), "s"(q)
    : "v244", "v245", "v246", "v247", "v248", "v249", "v250", "v251", "v252", "v253", "v254", "v255", "v243", "s98", "s99", "vcc", "scc", "memory");
}
__device__ __forceinline__ void tr_u(v4f (&u)[3], unsigned v0, unsigned v1, unsigned v2, int ix, const v8u (&w)[9], unsigned& t, unsigned& cnt, __amdgpu_buffer_rsrc_t rs, unsigned q) {
  asm volatile(
    "1:\n\t"
    "s_memtime s[98:99]\n\t"
    "s_waitcnt lgkmcnt(0)\n\t"
    "s_sub_u32 s99, %0, s98\n\t"
    "s_sub_u32 s99, s99, 24\n\t"
    "s_cmp_gt_u32 s99, 0x100000\n\t"
    "s_cbranch_scc1 5f\n\t"
    "s_cmp_lt_u32 s99, 96\n\t"
    "s_cbranch_scc1 1b\n\t"
    "s_sleep 1\n\t"
    "s_branch 1b\n\t"
    "5:\n\t"
    "buffer_load_dwordx4 v[244:247], %5, %18, 0 offen offset:0 sc1\n\t"
    "buffer_load_dwordx4 v[248:251], %6, %18, 0 offen offset:0 sc1\n\t"
    "buffer_load_dwordx4 v[252:255], %7, %18, 0 offen offset:0 sc1\n\t"
    "s_nop 7\n\t"
    "v_mov_b32 v228, 0\n\t"
    "v_mov_b32 v229, 0\n\t"
    "v_mov_b32 v230, 0\n\t"
    "v_mov_b32 v231, 0\n\t"
    "v_mov_b32 v232, 0\n\t"
    "v_mov_b32 v233, 0\n\t"
    "v_mov_b32 v234, 0\n\t"
    "v_mov_b32 v235, 0\n\t"
    "v_mov_b32 v236, 0\n\t"
    "v_mov_b32 v237, 0\n\t"
    "v_mov_b32 v238, 0\n\t"
    "v_mov_b32 v239, 0\n\t"
    "s_waitcnt vmcnt(2)\n\t"
    "v_max3_u32 v243, v244, v245, v246\n\t"
    "v_max_u32 v243, v243, v247\n\t"
    "v_smfmac_f32_16x16x64_bf16 v[228:231], v[244:247], %9, %8\n\t"
    "v_smfmac_f32_16x16x64_bf16 v[232:235], v[244:247], %12, %8\n\t"
    "v_smfmac_f32_16x16x64_bf16 v[236:239], v[244:247], %15, %8\n\t"
    "s_waitcnt vmcnt(1)\n\t"
    "v_max3_u32 v243, v243, v248, v249\n\t"
    "v_max3_u32 v243, v243, v250, v251\n\t"
    "v_smfmac_f32_16x16x64_bf16 v[228:231], v[248:251], %10, %8\n\t"
    "v_smfmac_f32_16x16x64_bf16 v[232:235], v[248:251], %13, %8\n\t"
    "v_smfmac_f32_16x16x64_bf16 v[236:239], v[248:251], %16, %8\n\t"
    "s_waitcnt vmcnt(0)\n\t"
    "v_max3_u32 v243, v243, v252, v253\n\t"
    "v_max3_u32 v243, v243, v254, v255\n\t"
    "v_cmp_eq_u32 vcc, -1, v243\n\t"
    "v_smfmac_f32_16x16x64_bf16 v[228:231], v[252:255], %11, %8\n\t"
    "v_smfmac_f32_16x16x64_bf16 v[232:235], v[252:255], %14, %8\n\t"
    "v_smfmac_f32_16x16x64_bf16 v[236:239], v[252:255], %17, %8\n\t"
    "s_cbranch_vccz 3f\n\t"
    "s_sub_u32 %1, %1, 1\n\t"
    "s_cmp_eq_u32 %1, 0\n\t"
    "s_cbranch_scc1 3f\n\t"
    "s_cmp_eq_u32 %19, 0\n\t"
    "s_cbranch_scc1 5b\n\t"
    "s_memtime s[98:99]\n\t"
    "s_waitcnt lgkmcnt(0)\n\t"
    "2:\n\t"
    "s_add_u32 %0, %0, %19\n\t"
    "s_sub_u32 s99, s98, %0\n\t"
    "s_cmp_gt_i32 s99, 0\n\t"
    "s_cbranch_scc1 2b\n\t"
    "s_branch 1b\n\t"
    "3:\n\t"
    "s_nop 6\n\t"
    : "+s"(t), "+s"(cnt), "=&{v[228:231]}"(u[0]), "=&{v[232:235]}"(u[1]), "=&{v[236:239]}"(u[2])
    : "v"(v0), "v"(v1), "v"(v2), "v"(ix), "v"(w[0]), "v"(w[1]), "v"(w[2]), "v"(w[3]), "v"(w[4]), "v"(w[5]), "v"(w[6]), "v"(w[7]), "v"(w[8]), "s"(rs), "s"(q)
    : "v244", "v245", "v246", "v247", "v248", "v249", "v250", "v251", "v252", "v253", "v254", "v255", "v243", "s98", "s99", "vcc", "scc", "memory");
}
__device__ __forceinline__ void tr_s0(v4f& acc, unsigned v0, unsigned v1, unsigned v2, unsigned& t, unsigned& cnt, __amdgpu_buffer_rsrc_t rs, unsigned q) {
  asm volatile(
    "1:\n\t"
    "s_memtime s[98:99]\n\t"
    "s_waitcnt lgkmcnt(0)\n\t"
    "s_sub_u32 s99, %0, s98\n\t"
    "s_sub_u32 s99, s99, 24\n\t"
    "s_cmp_gt_u32 s99, 0x100000\n\t"
    "s_cbranch_scc1 5f\n\t"
    "s_cmp_lt_u32 s99, 96\n\t"
    "s_cbranch_scc1 1b\n\t"
    "s_sleep 1\n\t"
    "s_branch 1b\n\t"
    "5:\n\t"
    "buffer_load_dwordx4 v[232:235], %3, %6, 0 offen offset:0 sc1\n\t"
    "buffer_load_dwordx4 v[236:239], %3, %6, 0 offen offset:128 sc1\n\t"
    "buffer_load_dwordx4 v[240:243], %4, %6, 0 offen offset:0 sc1\n\t"
    "buffer_load_dwordx4 v[244:247], %4, %6, 0 offen offset:128 sc1\n\t"
    "buffer_load_dwordx4 v[248:251], %5, %6, 0 offen offset:0 sc1\n\t"
    "buffer_load_dwordx4 v[252:255], %5, %6, 0 offen offset:128 sc1\n\t"
    "s_waitcnt vmcnt(5)\n\t"
    "v_max3_u32 v231, v232, v233, v234\n\t"
    "v_max_u32 v231, v231, v235\n\t"
    "v_mfma_f32_16x16x32_bf16 %2, v[232:235], v[232:235], 0\n\t"
    "s_waitcnt vmcnt(4)\n\t"
    "v_max3_u32 v231, v231, v236, v237\n\t"
    "v_max3_u32 v231, v231, v238, v239\n\t"
    "v_mfma_f32_16x16x32_bf16 %2, v[236:239], v[236:239], %2\n\t"
    "s_waitcnt vmcnt(3)\n\t"
    "v_max3_u32 v231, v231, v240, v241\n\t"
    "v_max3_u32 v231, v231, v242, v243\n\t"
    "v_mfma_f32_16x16x32_bf16 %2, v[240:243], v[240:243], %2\n\t"
    "s_waitcnt vmcnt(2)\n\t"
    "v_max3_u32 v231, v231, v244, v245\n\t"
    "v_max3_u32 v231, v231, v246, v247\n\t"
    "v_mfma_f32_16x16x32_bf16 %2, v[244:247], v[244:247], %2\n\t"
    "s_waitcnt vmcnt(1)\n\t"
    "v_max3_u32 v231, v231, v248, v249\n\t"
    "v_max3_u32 v231, v231, v250, v251\n\t"
    "v_mfma_f32_16x16x32_bf16 %2, v[248:251], v[248:251], %2\n\t"
    "s_waitcnt vmcnt(0)\n\t"
    "v_max3_u32 v231, v231, v252, v253\n\t"
    "v_max3_u32 v231, v231, v254, v255\n\t"
    "v_cmp_eq_u32 vcc, -1, v231\n\t"
    "v_mfma_f32_16x16x32_bf16 %2, v[252:255], v[252:255], %2\n\t"
    "s_cbranch_vccz 3f\n\t"
    "s_sub_u32 %1, %1, 1\n\t"
    "s_cmp_eq_u32 %1, 0\n\t"
    "s_cbranch_scc1 3f\n\t"
    "s_cmp_eq_u32 %7, 0\n\t"
    "s_cbranch_scc1 5b\n\t"
    "s_memtime s[98:99]\n\t"
    "s_waitcnt lgkmcnt(0)\n\t"
    "2:\n\t"
    "s_add_u32 %0, %0, %7\n\t"
    "s_sub_u32 s99, s98, %0\n\t"
    "s_cmp_gt_i32 s99, 0\n\t"
    "s_cbranch_scc1 2b\n\t"
    "s_branch 1b\n\t"
    "3:\n\t"
    "s_nop 6\n\t"
    : "+s"(t), "+s"(cnt), "=&v"(acc)
    : "v"(v0), "v"(v1), "v"(v2), "s"(rs), "s"(q)
    : "v232", "v233", "v234", "v235", "v236", "v237", "v238", "v239", "v240", "v241", "v242", "v243", "v244", "v245", "v246", "v247", "v248", "v249", "v250", "v251", "v252", "v253", "v254", "v255", "v231", "s98", "s99", "vcc", "scc", "memory");
}
__device__ __forceinline__ void tr_sx(v4f& aa, v4f& ab, unsigned v0, unsigned v1, unsigned v2, const v4u (&k)[6], unsigned& t, unsigned& cnt, __amdgpu_buffer_rsrc_t rs, unsigned q) {
  asm volatile(
    "1:\n\t"
    "s_memtime s[98:99]\n\t"
    "s_waitcnt lgkmcnt(0)\n\t"
    "s_sub_u32 s99, %0, s98\n\t"
    "s_sub_u32 s99, s99, 24\n\t"
    "s_cmp_gt_u32 s99, 0x100000\n\t"
    "s_cbranch_scc1 5f\n\t"
    "s_cmp_lt_u32 s99, 96\n\t"
    "s_cbranch_scc1 1b\n\t"
    "s_sleep 1\n\t"
    "s_branch 1b\n\t"
    "5:\n\t"
    "buffer_load_dwordx4 v[244:247], %4, %13, 0 offen offset:0 sc1\n\t"
    "buffer_load_dwordx4 v[248:251], %5, %13, 0 offen offset:0 sc1\n\t"
    "buffer_load_dwordx4 v[252:255], %6, %13, 0 offen offset:0 sc1\n\t"
    "s_waitcnt vmcnt(2)\n\t"
    "v_max3_u32 v243, v244, v245, v246\n\t"
    "v_max_u32 v243, v243, v247\n\t"
    "v_mfma_f32_16x16x32_bf16 %2, %7, v[244:247], 0\n\t"
    "v_mfma_f32_16x16x32_bf16 %3, %8, v[244:247], 0\n\t"
    "s_waitcnt vmcnt(1)\n\t"
    "v_max3_u32 v243, v243, v248, v249\n\t"
    "v_max3_u32 v243, v243, v250, v251\n\t"
    "v_mfma_f32_16x16x32_bf16 %2, %9, v[248:251], %2\n\t"
    "v_mfma_f32_16x16x32_bf16 %3, %10, v[248:251], %3\n\t"
    "s_waitcnt vmcnt(0)\n\t"
    "v_max3_u32 v243, v243, v252, v253\n\t"
    "v_max3_u32 v243, v243, v254, v255\n\t"
    "v_cmp_eq_u32 vcc, -1, v243\n\t"
    "v_mfma_f32_16x16x32_bf16 %2, %11, v[252:255], %2\n\t"
    "v_mfma_f32_16x16x32_bf16 %3, %12, v[252:255], %3\n\t"
    "s_cbranch_vccz 3f\n\t"
    "s_sub_u32 %1, %1, 1\n\t"
    "s_cmp_eq_u32 %1, 0\n\t"
    "s_cbranch_scc1 3f\n\t"
    "s_cmp_eq_u32 %14, 0\n\t"
    "s_cbranch_scc1 5b\n\t"
    "s_memtime s[98:99]\n\t"
    "s_waitcnt lgkmcnt(0)\n\t"
    "2:\n\t"
    "s_add_u32 %0, %0, %14\n\t"
    "s_sub_u32 s99, s98, %0\n\t"
    "s_cmp_gt_i32 s99, 0\n\t"
    "s_cbranch_scc1 2b\n\t"
    "s_branch 1b\n\t"
    "3:\n\t"
    "s_nop 6\n\t"
    : "+s"(t), "+s"(cnt), "=&v"(aa), "=&v"(ab)
    : "v"(v0), "v"(v1), "v"(v2), "v"(k[0]), "v"(k[1]), "v"(k[2]), "v"(k[3]), "v"(k[4]), "v"(k[5]), "s"(rs), "s"(q)
    : "v244", "v245", "v246", "v247", "v248", "v249", "v250", "v251", "v252", "v253", "v254", "v255", "v243", "s98", "s99", "vcc", "scc", "memory");
}
__device__ __forceinline__ void tr_red(v2f& sa, v2f& sb, unsigned v0, unsigned v1, unsigned& t, unsigned& cnt, __amdgpu_buffer_rsrc_t rs, unsigned q) {
  asm volatile(
    "1:\n\t"
    "s_memtime s[98:99]\n\t"
    "s_waitcnt lgkmcnt(0)\n\t"
    "s_sub_u32 s99, %0, s98\n\t"
    "s_sub_u32 s99, s99, 24\n\t"
    "s_cmp_gt_u32 s99, 0x100000\n\t"
    "s_cbranch_scc1 5f\n\t"
    "s_cmp_lt_u32 s99, 96\n\t"
    "s_cbranch_scc1 1b\n\t"
    "s_sleep 1\n\t"
    "s_branch 1b\n\t"
    "5:\n\t"
    "buffer_load_dwordx4 v[224:227], %4, %6, 0 offen offset:0 sc1\n\t"
    "buffer_load_dwordx4 v[228:231], %4, %6, 0 offen offset:1024 sc1\n\t"
    "buffer_load_dwordx4 v[232:235], %4, %6, 0 offen offset:2048 sc1\n\t"
    "buffer_load_dwordx4 v[236:239], %4, %6, 0 offen offset:3072 sc1\n\t"
    "buffer_load_dwordx4 v[240:243], %5, %6, 0 offen offset:0 sc1\n\t"
    "buffer_load_dwordx4 v[244:247], %5, %6, 0 offen offset:1024 sc1\n\t"
    "buffer_load_dwordx4 v[248:251], %5, %6, 0 offen offset:2048 sc1\n\t"
    "buffer_load_dwordx4 v[252:255], %5, %6, 0 offen offset:3072 sc1\n\t"
    "s_waitcnt vmcnt(7)\n\t"
    "v_max_u32 v223, v224, v226\n\t"
    "s_waitcnt vmcnt(6)\n\t"
    "v_max3_u32 v223, v223, v228, v230\n\t"
    "v_pk_add_f32 %2, v[224:225], v[228:229]\n\t"
    "v_pk_add_f32 %3, v[226:227], v[230:231]\n\t"
    "s_waitcnt vmcnt(5)\n\t"
    "v_max3_u32 v223, v223, v232, v234\n\t"
    "v_pk_add_f32 %2, %2, v[232:233]\n\t"
    "v_pk_add_f32 %3, %3, v[234:235]\n\t"
    "s_waitcnt vmcnt(4)\n\t"
    "v_max3_u32 v223, v223, v236, v238\n\t"
    "v_pk_add_f32 %2, %2, v[236:237]\n\t"
    "v_pk_add_f32 %3, %3, v[238:239]\n\t"
    "s_waitcnt vmcnt(3)\n\t"
    "v_max3_u32 v223, v223, v240, v242\n\t"
    "v_pk_add_f32 %2, %2, v[240:241]\n\t"
    "v_pk_add_f32 %3, %3, v[242:243]\n\t"
    "s_waitcnt vmcnt(2)\n\t"
    "v_max3_u32 v223, v223, v244, v246\n\t"
    "v_pk_add_f32 %2, %2, v[244:245]\n\t"
    "v_pk_add_f32 %3, %3, v[246:247]\n\t"
    "s_waitcnt vmcnt(1)\n\t"
    "v_max3_u32 v223, v223, v248, v250\n\t"
    "v_pk_add_f32 %2, %2, v[248:249]\n\t"
    "v_pk_add_f32 %3, %3, v[250:251]\n\t"
    "s_waitcnt vmcnt(0)\n\t"
    "v_max3_u32 v223, v223, v252, v254\n\t"
    "v_cmp_eq_u32 vcc, -1, v223\n\t"
    "v_pk_add_f32 %2, %2, v[252:253]\n\t"
    "v_pk_add_f32 %3, %3, v[254:255]\n\t"
    "s_cbranch_vccz 3f\n\t"
    "s_sub_u32 %1, %1, 1\n\t"
    "s_cmp_eq_u32 %1, 0\n\t"
    "s_cbranch_scc1 3f\n\t"
    "s_cmp_eq_u32 %7, 0\n\t"
    "s_cbranch_scc1 5b\n\t"
    "s_memtime s[98:99]\n\t"
    "s_waitcnt lgkmcnt(0)\n\t"
    "2:\n\t"
    "s_add_u32 %0, %0, %7\n\t"
    "s_sub_u32 s99, s98, %0\n\t"
    "s_cmp_gt_i32 s99, 0\n\t"
    "s_cbranch_scc1 2b\n\t"
    "s_branch 1b\n\t"
    "3:\n\t"
    "s_nop 1\n\t"
    : "+s"(t), "+s"(cnt), "=&v"(sa), "=&v"(sb)
    : "v"(v0), "v"(v1), "s"(rs), "s"(q)
    : "v224", "v225", "v226", "v227", "v228", "v229", "v230", "v231", "v232", "v233", "v234", "v235", "v236", "v237", "v238", "v239", "v240", "v241", "v242", "v243", "v244", "v245", "v246", "v247", "v248", "v249", "v250", "v251", "v252", "v253", "v254", "v255", "v223", "s98", "s99", "vcc", "scc", "memory");
}

#define POLL_CHECK(st) do { if ((st) == 0u && maxit != 1u) { __hip_atomic_store(abortw, 1u, RLX_AGENT); maxit = 1u; } } while (0)
__device__ __forceinline__ unsigned x3phys(int s, int blk, unsigned par) {
  return blk < 8 ? ((unsigned)s << 14) + (par << 13) + blk * 1024u : 0x110000u + ((unsigned)(s >> 2) << 14) + (par << 13) + (s & 3) * 2048u + (blk - 8) * 1024u;
}
#define STAGE_SCHED(n) do { __builtin_amdgcn_sched_group_barrier(0x100, 10, 0); __builtin_amdgcn_sched_group_barrier(0x8, n, 0); } while (0)

__device__ __forceinline__ unsigned packA(v4f d, bool odd) {
  const float s01 = swap32_add(d[0], d[1]), s23 = swap32_add(d[2], d[3]);
  const float n01 = dpp_xor1(s01), n23 = dpp_xor1(s23);
  return min(pk2(odd ? n23 : s01, odd ? s23 : n01), 0xFFFFFFFEu);
}

__device__ __forceinline__ void vx_store(LAS unsigned char* tile, int kk, int lane, v4u v) {
  const int g = lane >> 4, key = lane & 15;
  LAS unsigned char* base = tile + ((kk >> 1) * 2 + (g >> 1)) * 1024 + ((2 * (g & 1)) * 16 + key) * 16 + (kk & 1) * 4;
  *(LAS unsigned*)(base) = v.x; *(LAS unsigned*)(base + 8) = v.y;
  *(LAS unsigned*)(base + 256) = v.z; *(LAS unsigned*)(base + 264) = v.w;
}
__device__ __forceinline__ v4u lo4(v8u w) { return __builtin_shufflevector(w, w, 0, 1, 2, 3); }
__device__ __forceinline__ v4u hi4(v8u w) { return __builtin_shufflevector(w, w, 4, 5, 6, 7); }

template <int V> struct IC { static constexpr int value = V; };
__device__ __forceinline__ unsigned char* xch_base(unsigned char* ws) {
  return (unsigned char*)((((uintptr_t)(ws + WS_XCH)) + ((2u << 20) - 1)) & ~(uintptr_t)((2u << 20) - 1));
}
__global__ void __launch_bounds__(512, 2) mega_kernel(MegaArgs a) {
  extern __shared__ __attribute__((aligned(16))) unsigned char lds_[];
  LAS unsigned char* lds = (LAS unsigned char*)lds_;
  const int b = blockIdx.x;
  if ((b & 7) >= 4) {
    const int w = (b & 3) * 32 + (b >> 3);
    WSTAMP(0);
    if (w >= 96) DELAY(a.stag);
    else prefix_role(a, lds, w);
    WSTAMP(5);
    if (w < 48 || (w >= 96 && w < 126)) proj_role(a, lds, w < 48 ? w : w - 48, w < 48 ? 0 : 2);
    __syncthreads();
    WSTAMP(1);
    int o = -1;
    if (w >= 48 && w < 73) o = w - 48; else if (w < 26) o = 25 + w; else if (w >= 96 && w < 120) o = 51 + (w - 96);
    if (o >= 0) out_role(a, lds, (o % 25) / 5, o % 5, o / 25);
    else if (w >= 73 && w < 89) att_role(a, lds, w - 73);
    else if ((w >= 89 && w < 96) || w >= 126) fin_role(a, w < 96 ? w - 89 : w - 119, 9);
    WSTAMP(2);
    return;
  }
  const int tid = threadIdx.x, lane = tid & 63, wave = __builtin_amdgcn_readfirstlane(tid >> 6);
  const int cu = (b & 3) * 32 + (b >> 3), h = cu >> 2, j = cu & 3;
  const int g = lane >> 4, r16 = lane & 15;
  gu32* ctl = (gu32*)(a.ws + WS_CTL);
  gu32* abortw = ctl;
  const unsigned par3 = ctl[16], par1 = ctl[17], par2a = ctl[18], par2b = ctl[19];
  const __amdgpu_buffer_rsrc_t rx = __builtin_amdgcn_make_buffer_rsrc(xch_base(a.ws), 0, (int)XCH_BYTES, 0x00020000);
  const __amdgpu_buffer_rsrc_t rm0 = __builtin_amdgcn_make_buffer_rsrc((void*)a.mem0, 0, MM * DM * 2, 0x00020000);
  const __amdgpu_buffer_rsrc_t rK = __builtin_amdgcn_make_buffer_rsrc(a.ws + WS_K, 0, S_ * HD * 2, 0x00020000);
  const __amdgpu_buffer_rsrc_t rV = __builtin_amdgcn_make_buffer_rsrc(a.ws + WS_V, 0, S_ * HD * 2, 0x00020000);
  const __amdgpu_buffer_rsrc_t rKM = __builtin_amdgcn_make_buffer_rsrc(a.ws + WS_KM, 0, NSEG * NH * 9216, 0x00020000);
  unsigned maxit = SPIN_LIMIT;
  auto ldkv = [&](const __amdgpu_buffer_rsrc_t& r, int rt, int i) __attribute__((always_inline)) -> v4u {
    return __builtin_amdgcn_raw_buffer_load_b128(r, (unsigned)((rt * (HD / 32) + h * 6 + i) * 1024 + lane * 16), 0, 16); };
  auto wait_kv = [&](int sg) __attribute__((always_inline)) {
    for (unsigned sp = 0; sp < maxit; ++sp) {
      if (__builtin_amdgcn_readfirstlane(__hip_atomic_load(CNT(ctl, CTL_KV + sg), RLX_AGENT)) >= 96u) { asm volatile("" ::: "memory"); return; }
      __builtin_amdgcn_s_sleep(8);
    }
    if (maxit != 1u) { __hip_atomic_store(abortw, 1u, RLX_AGENT); maxit = 1u; }
    asm volatile("" ::: "memory");
  };
  const bool is_red = j < 2 || (j == 2 && !(h & 1));
  const int U = j < 2 ? 2 * h + j : 64 + (h >> 1);
  const int sidx = r16 < 8 ? 0x4444 : 0xEEEE;
  const unsigned ldsbase = (unsigned)(size_t)lds;
  const bool odd = lane & 1;

  auto stage_w = [&](int rd) __attribute__((always_inline)) {
#pragma unroll
    for (int i = 0; i < 15; ++i) {
      const int blk = i * 8 + wave, c0 = blk * 64 + lane;
      const u16* src;
      if (blk < 60) { const u16* W = rd == 0 ? a.Wq : a.Wv; src = W + (size_t)(c0 / 6) * HD + h * DH + 48 * j + (c0 % 6) * 8; }
      else { const int c = c0 - 3840;
        if (rd == 0) src = a.Wk + (size_t)(c / 6) * HD + h * DH + 48 * j + (c % 6) * 8;
        else src = a.Wo + (size_t)(h * DH + c / 20) * DM + 160 * j + (c % 20) * 8; }
      glds16(src, lds + blk * 1024);
    }
    asm volatile("s_waitcnt vmcnt(0)" ::: "memory");
    __syncthreads();
  };
  auto load_tile = [&](v8u (&w)[10], int tl) __attribute__((always_inline)) {
    const int m = tl / 3, sb = tl % 3;
#pragma unroll
    for (int pp = 0; pp < 10; ++pp) { w[pp] = tr_frag64(lds + (m == 1 ? 61440 : 0), 96, pp * 64, sb * 16 + 4 * (lane & 3), lane); asm volatile("" : "+v"(w[pp])); }
  };
  auto kv_dma = [&](int sg, int blk) __attribute__((always_inline)) {
    const int bb = blk % 12;
    const unsigned char* src = a.ws + (blk < 12 ? WS_K : WS_V) + ((size_t)(sg * 2 + bb / 6) * (HD / 32) + h * 6 + bb % 6) * 1024 + lane * 16;
    glds16_sc1(src, lds + (blk < 12 ? CL_KX : CL_VX + (sg & 1) * 12288) + bb * 1024);
  };
  auto init_lds = [&]() __attribute__((always_inline)) {
    __syncthreads();
    if (tid < 8) *(LAS unsigned*)(lds + CL_FLAG + tid * 4) = 0u;
    wait_kv(0);
#pragma unroll
    for (int i = 0; i < 3; ++i) kv_dma(0, wave * 3 + i);
    asm volatile("s_waitcnt vmcnt(0)" ::: "memory");
    __syncthreads();
  };
  auto x1off = [&](int tl) __attribute__((always_inline)) -> unsigned {
    const int e = 48 * j + 16 * (tl % 3) + (r16 & ~1);
    const int row = 4 * (g & 1) + (lane >> 5) + (odd ? 2 : 0);
    return (unsigned)(((tl / 3) * 3 + (e >> 6)) * 1024 + (((e >> 3) & 3) * 16 + ((e >> 5) & 1) * 8 + row) * 16 + (e & 7) * 2);
  };
  const unsigned X1B = 2u << 20, X2B0 = 4u << 20, X2B1 = 6u << 20;
  auto x1addr = [&](int slot, unsigned o) __attribute__((always_inline)) -> unsigned { return X1B + xmap((unsigned)((slot * 32 + h) * 9216) + o, par1); };
  const LAS unsigned char* memb = lds + CL_MEM + lane * 16;
  const float cexp = 0.07216878364870322f * 1.4426950408889634f;
#define NOW() ((unsigned)__builtin_amdgcn_s_memtime())
  const v2u ones_x = {0x3F803F80u, 0x3F803F80u}, ones_m = g < 2 ? ones_x : (v2u){0u, 0u};
  auto final_tiles = [&](auto ntag, const v2u* uf, const v2u (*yf)[2], unsigned x2a) __attribute__((always_inline)) {
    constexpr int NTL = decltype(ntag)::value;
    const v4u p0 = *(const LAS v4u*)(lds + CL_PB + (lane | 32) * 16);
    const v4u p1 = *(const LAS v4u*)(lds + CL_PB + 1024 + lane * 16);
    const v4u p2 = *(const LAS v4u*)(lds + CL_PB + 2048 + lane * 16);
    const v4f l0 = mfma16z(ones_m, (v2u){p0.x, p0.y}), l1 = mfma16z(ones_x, (v2u){p1.x, p1.y}), l2 = mfma16z(ones_x, (v2u){p2.x, p2.y});
    v4f o[NTL][3];
#pragma unroll
    for (int t = 0; t < NTL; ++t) {
      o[t][0] = mfma16z(uf[t], (v2u){p0.x, p0.y});
      o[t][1] = mfma16z(yf[t][0], (v2u){p1.x, p1.y});
      o[t][2] = mfma16z(yf[t][1], (v2u){p2.x, p2.y});
    }
    const float mm = fmax3(u2f(p0.z), u2f(p1.z), u2f(p2.z));
    float f0 = __builtin_amdgcn_exp2f(u2f(p0.z) - mm), f1 = __builtin_amdgcn_exp2f(u2f(p1.z) - mm), f2 = __builtin_amdgcn_exp2f(u2f(p2.z) - mm);
    const float rinv = __builtin_amdgcn_rcpf(f0 * l0[0] + f1 * l1[0] + f2 * l2[0]);
    f0 *= rinv; f1 *= rinv; f2 *= rinv;
    v4u ov[NTL];
#pragma unroll
    for (int t = 0; t < NTL; ++t) {
      const v4f r = o[t][0] * f0 + o[t][1] * f1 + o[t][2] * f2;
      ov[t] = (v4u){nosent(f2u(r[0])), f2u(r[1]), nosent(f2u(r[2])), f2u(r[3])};
    }
    if (r16 < 8) {
#pragma unroll
      for (int t = 0; t < NTL; ++t) stx16(rx, x2a + t * 32768u, ov[t]);
    }
  };
  auto x2addr = [&](int T, int slot) __attribute__((always_inline)) -> unsigned {
    return (unsigned)(((2 * (10 * j + T) + (g >> 1)) << 14) + h * 256 + ((r16 & 7) * 2 + (g & 1)) * 16) + (slot ? X2B1 + (par2b << 13) : X2B0 + (par2a << 13)); };

  auto a_role = [&](auto tag) __attribute__((always_inline)) {
    constexpr int ROLE = decltype(tag)::value;
    v8u wA[10], wB[10];
    stage_w(0); if (ROLE < 3) { load_tile(wA, 2 * wave); load_tile(wB, 2 * wave + 1); } __syncthreads();
    stage_w(1); if (ROLE == 3) { load_tile(wA, 6); load_tile(wB, 7); }
    init_lds();
    const unsigned poA = x1off(2 * wave), poB = x1off(2 * wave + 1);
    const bool keepA = wave >= 2, keepB = ROLE >= 1;
    const bool red = ROLE == 0 && is_red;
    const unsigned x2s0 = X2B0 + (par2a << 13) + ((unsigned)U << 14), x2s1 = X2B1 + (par2b << 13) + ((unsigned)U << 14);
    const unsigned x3o = (unsigned)(((U & 3) * 16 + 8 * ((U & 7) >> 2)) * 16 + (r16 >> 1) * 16 + (r16 & 1) * 8 + (lane >> 5) * 4);
    const int kt = wave - 1;
    const unsigned rdo = ROLE == 0 ? (unsigned)((r16 >> 3) * 3072 + (g * 16 + (r16 & 7)) * 16) : (unsigned)(lane * 16);
    __builtin_amdgcn_s_setprio(1);
    STAMP_DECL;
    int sl = 0;
    for (int s = 0; s < NSEG; ++s) {
      const int slp = sl == 0 ? 2 : sl - 1;
      const unsigned pubA = x1addr(sl, poA), pubB = x1addr(sl, poB);
      unsigned qo[3];
#pragma unroll
      for (int pp = 0; pp < 3; ++pp) qo[pp] = x1addr(sl, rdo + pp * 1024);
      __builtin_amdgcn_sched_barrier(0);
      __builtin_amdgcn_s_waitcnt(0xC07F); __builtin_amdgcn_s_barrier();
      asm volatile("" ::: "memory");
      CST(0);
      v4f dA = {0, 0, 0, 0}, dB = dA;
#pragma unroll
      for (int pp = 0; pp < 10; ++pp) { const v4u x = *(const LAS v4u*)(memb + pp * 1024); dA = smfma(x, wA[pp], dA, sidx); dB = smfma(x, wB[pp], dB, sidx); }
      __builtin_amdgcn_sched_group_barrier(0x100, 6, 0);
#pragma unroll
      for (int pp = 0; pp < 4; ++pp) { __builtin_amdgcn_sched_group_barrier(0x8, 2, 0); __builtin_amdgcn_sched_group_barrier(0x100, 1, 0); }
      __builtin_amdgcn_sched_group_barrier(0x8, 12, 0);
      const unsigned pkA = packA(dA, odd), pkB = packA(dB, odd);
      stx4(rx, pubA, 0, pkA);
      stx4(rx, pubB, 0, pkB);
      if (keepA) stx4(rKM, (unsigned)((s * NH + h) * 9216) + poA, 0, pkA);
      if (keepB) stx4(rKM, (unsigned)((s * NH + h) * 9216) + poB, 0, pkB);
      __builtin_amdgcn_sched_barrier(0);
      unsigned tt = NOW() + 24 * (ROLE == 0 ? a.d1 : a.d1q), st = maxit;
      STAMPL(1);
      CST(1);
      if (s == NSEG - 1) break;
      v4u yh = {0, 0, 0, 0};
      const unsigned x2h = x2addr(3 * kt + 2, s & 1);
      __builtin_amdgcn_s_setprio(3);
      if (ROLE == 0) {
        v4f t;
        tr_s0(t, qo[0], qo[1], qo[2], tt, st, rx, 0u);
        CST(2);
        const float mc = swap16_max(fmax2(fmax3(t[0], t[1], t[2]), t[3])) * cexp;
        float p[4];
#pragma unroll
        for (int r = 0; r < 4; ++r) p[r] = __builtin_amdgcn_exp2f(t[r] * cexp - mc);
        *(LAS v4u*)(lds + CL_PB + lane * 16) = (v4u){pk2(p[0], p[1]), pk2(p[2], p[3]), f2u(mc), 0u};
      } else if (ROLE == 1) {
        yh = *(const LAS v4u*)(lds + CL_UY + kt * 2048 + lane * 16);
        v4u kx[6];
#pragma unroll
        for (int i = 0; i < 6; ++i) kx[i] = *(const LAS v4u*)(lds + CL_KX + (kt * 6 + i) * 1024 + lane * 16);
        v4f t0, t1;
        tr_sx(t0, t1, qo[0], qo[1], qo[2], kx, tt, st, rx, 0u);
        CST(2);
        float t[4];
#pragma unroll
        for (int r = 0; r < 4; ++r) t[r] = t0[r] + dpp_shl8(t1[r]);
        const float mc = swap32_max(swap16_max(fmax2(fmax3(t[0], t[1], t[2]), t[3]))) * cexp;
        float p[4];
#pragma unroll
        for (int r = 0; r < 4; ++r) p[r] = __builtin_amdgcn_exp2f(t[r] * cexp - mc);
        *(LAS v4u*)(lds + CL_PB + wave * 1024 + lane * 16) = (v4u){pk2(p[0], p[1]), pk2(p[2], p[3]), f2u(mc), 0u};
      }
      if (ROLE == 3) {
        if (s == 0) { wait_kv(1);
#pragma unroll
          for (int i = 0; i < 12; ++i) kv_dma(1, 12 + i); }
        asm volatile("s_waitcnt vmcnt(0)" ::: "memory");
      }
      asm volatile("s_waitcnt lgkmcnt(0)" ::: "memory");
      CST(3);
      __builtin_amdgcn_s_waitcnt(0xC07F); __builtin_amdgcn_s_barrier();
      asm volatile("" ::: "memory");
      __builtin_amdgcn_sched_barrier(0);
      CST(4);
      const unsigned tb2 = NOW() + 24u * (unsigned)(is_red ? (a.pad & 255) : (a.pad >> 8));
      if (ROLE == 1) {
        const v2u uh[1] = {*(const LAS v2u*)(lds + CL_UY + kt * 2048 + 1024 + lane * 8)};
        const v2u yy[1][2] = {{(v2u){yh.x, yh.y}, (v2u){yh.z, yh.w}}};
        final_tiles(IC<1>{}, uh, yy, x2h);
        __builtin_amdgcn_sched_barrier(0);
      }
      POLL_CHECK(st);
      if (red) {
        const unsigned so = ((s & 1) ? x2s1 : x2s0) + lane * 16;
        const unsigned x3a = x3phys(s + 1, U >> 3, par3) + x3o;
        v2f sa, sb;
        unsigned t2 = NOW() + 24 * a.d2, st2 = maxit;
        tr_red(sa, sb, so, so + 4096u, t2, st2, rx, 0u);
        CST(5);
        const float va = swap16_add(swap32_add(sa[0], sb[0])), vb = swap16_add(swap32_add(sa[1], sb[1]));
        const unsigned w = min(pk2(va, vb), 0xFFFFFFFEu);
        if ((g & 1) == 0) stx4(rx, x3a, 0, w);
        __builtin_amdgcn_sched_barrier(0);
        STAMPL(6);
        CST(6);
        STAMPV(7, maxit - st2);
        POLL_CHECK(st2);
      }
      __builtin_amdgcn_s_setprio(0);
      if (ROLE == 3 && lane == 0) __hip_atomic_fetch_add(CNT(ctl, CTL_KM + s * NH + h), 1u, RLX_AGENT);
      if (ROLE > 0) {
        if (s == 0) { wait_kv(1); wait_kv(2); }
        while ((int)(tb2 - NOW()) > 0) __builtin_amdgcn_s_sleep(1);
#pragma unroll
        for (int i = 0; i < 8; ++i) { const int blk = (wave - 1) * 8 + i; if (blk < 12 || s + 2 < NSEG) kv_dma(blk < 12 ? s + 1 : s + 2, blk); }
      }
      stx4(rx, x1addr(slp, poA), 0, SENT); stx4(rx, x1addr(slp, poB), 0, SENT);
      if (ROLE > 0) {
        if (s + 3 < NSEG) wait_kv(s + 3);
        asm volatile("s_waitcnt vmcnt(0)" ::: "memory");
      }
      __builtin_amdgcn_s_setprio(1);
      STAMP_FLUSH(s);
      sl = sl == 2 ? 0 : sl + 1;
    }
  };
  if (wave == 0) a_role(IC<0>{});
  else if (wave < 3) a_role(IC<1>{});
  else if (wave == 3) a_role(IC<3>{});
  else {
    const int cw = wave - 4;
    const int b0 = cw < 2 ? 3 * cw : 2 * cw + 2, nb = cw < 2 ? 3 : 2;
    v8u wK[3], wO[9];
    stage_w(0); __syncthreads();
    stage_w(1);
#pragma unroll
    for (int i = 0; i < 3; ++i) {
      wK[i] = tr_frag64(lds, 96, (b0 + (i < nb ? i : 0)) * 64, 32 + 4 * (lane & 3), lane);
      if (i >= nb) wK[i] = (v8u){0, 0, 0, 0, 0, 0, 0, 0};
      asm volatile("" : "+v"(wK[i]));
#pragma unroll
      for (int pp = 0; pp < 3; ++pp) { wO[i * 3 + pp] = tr_frag64(lds + 61440, 320, pp * 64, (b0 + (i < nb ? i : 0)) * 16 + 4 * (lane & 3), lane); asm volatile("" : "+v"(wO[i * 3 + pp])); }
    }
    init_lds();
    v2u yf[3][2];
    auto computeY = [&](int buf) __attribute__((always_inline)) {
      const LAS unsigned char* vb = lds + CL_VX + buf * 12288 + (r16 >> 3) * 1024 + (g * 16 + (r16 & 7)) * 16;
#pragma unroll
      for (int k = 0; k < 2; ++k) {
        v4f y[3][2];
#pragma unroll
        for (int t = 0; t < 3; ++t) { y[t][0] = (v4f){0, 0, 0, 0}; y[t][1] = (v4f){0, 0, 0, 0}; }
#pragma unroll
        for (int pp = 0; pp < 3; ++pp) {
          const v4u v0 = *(const LAS v4u*)(vb + (k * 6 + 2 * pp) * 1024), v1 = *(const LAS v4u*)(vb + (k * 6 + 2 * pp) * 1024 + 128);
#pragma unroll
          for (int t = 0; t < 3; ++t) { y[t][0] = smfma(v0, wO[t * 3 + pp], y[t][0], sidx); y[t][1] = smfma(v1, wO[t * 3 + pp], y[t][1], sidx); }
        }
#pragma unroll
        for (int t = 0; t < 3; ++t) {
          float z[4];
#pragma unroll
          for (int r = 0; r < 4; ++r) z[r] = swap32_add(y[t][0][r], y[t][1][r]);
          yf[t][k] = (v2u){pk2(z[0], z[1]), pk2(z[2], z[3])}; asm volatile("" : "+v"(yf[t][k]));
        }
      }
      if (cw < 2) *(LAS v4u*)(lds + CL_UY + cw * 2048 + (buf & 1) * 0 + lane * 16) = (v4u){yf[2][0].x, yf[2][0].y, yf[2][1].x, yf[2][1].y};
    };
    computeY(0);
    const unsigned x2o = (unsigned)(((2 * (10 * j + b0) + (g >> 1)) << 14) + h * 256 + ((r16 & 7) * 2 + (g & 1)) * 16);
    const unsigned x2s0 = X2B0 + (par2a << 13), x2s1 = X2B1 + (par2b << 13);
    const unsigned m0o = (unsigned)(((r16 & 7) * DM + 32 * (r16 >> 3) + 8 * g) * 2);
    const unsigned po9 = x1off(8);
    unsigned tdl = 0;
    __builtin_amdgcn_s_setprio(2);
    STAMP_DECL;
    int sl = 0;
    for (int s = 0; s < NSEG; ++s) {
      const int slp = sl == 0 ? 2 : sl - 1;
      const unsigned x2a = x2o + ((s & 1) ? x2s1 : x2s0);
      const unsigned pub9 = x1addr(sl, po9);
      unsigned vo[3];
#pragma unroll
      for (int pp = 0; pp < 3; ++pp) vo[pp] = x1addr(sl, (unsigned)((6 + pp) * 1024 + lane * 16));
      unsigned stm = maxit;
      if (s == 0) {
        for (int i = 0; i < nb; ++i)
          *(LAS v4u*)(lds + CL_MEM + (b0 + i) * 1024 + lane * 16) = __builtin_amdgcn_raw_buffer_load_b128(rm0, m0o, (b0 + i) * 128, 0);
        asm volatile("s_waitcnt lgkmcnt(0)" ::: "memory");
      } else {
        const unsigned o0 = x3phys(s, b0, par3) + lane * 16;
        if (nb == 3) tr_mem3(o0, ldsbase + CL_MEM + b0 * 1024 + lane * 16, tdl, stm, rx, 0u);
        else tr_mem2(o0, ldsbase + CL_MEM + b0 * 1024 + lane * 16, tdl, stm, rx, 0u);
      }
      CST(7);
      __builtin_amdgcn_s_waitcnt(0xC07F); __builtin_amdgcn_s_barrier();
      asm volatile("" ::: "memory");
      CST(0);
      v4f d9 = {0, 0, 0, 0};
#pragma unroll
      for (int i = 0; i < 3; ++i) d9 = smfma(*(const LAS v4u*)(memb + (b0 + (i < nb ? i : 0)) * 1024), wK[i], d9, sidx);
      unsigned pk9 = 0;
      if (cw < 3) {
        *(LAS v4f*)(lds + CL_P9 + cw * 1024 + lane * 16) = d9;
        asm volatile("s_waitcnt lgkmcnt(0)" ::: "memory");
        *(volatile LAS unsigned*)(lds + CL_FLAG + cw * 4) = (unsigned)(s + 1);
      } else {
        for (;;) { const v4u f = *(volatile LAS v4u*)(lds + CL_FLAG); if (f.x == (unsigned)(s + 1) && f.y == (unsigned)(s + 1) && f.z == (unsigned)(s + 1)) break; }
        asm volatile("" ::: "memory");
        d9 += (*(const LAS v4f*)(lds + CL_P9 + lane * 16) + *(const LAS v4f*)(lds + CL_P9 + 1024 + lane * 16)) + *(const LAS v4f*)(lds + CL_P9 + 2048 + lane * 16);
        pk9 = packA(d9, odd);
        stx4(rx, pub9, 0, pk9);
        stx4(rKM, (unsigned)((s * NH + h) * 9216) + po9, 0, pk9);
      }
      __builtin_amdgcn_sched_barrier(0);
      unsigned tt = NOW() + 24 * (cw == 3 ? a.d1v : a.d1v + a.d4), stv_ = maxit;
      STAMPL(3);
      CST(1);
      POLL_CHECK(stm);
      if (cw == 1 && s > 0 && is_red) {
        const unsigned so = (((s - 1) & 1) ? x2s1 : x2s0) + ((unsigned)U << 14) + lane * 16;
#pragma unroll
        for (int i = 0; i < 8; ++i) stx16(rx, so + i * 1024, (v4u){SENT, SENT, SENT, SENT});
      }
      if (s == NSEG - 1) break;
      v4f u[3];
      tr_u(u, vo[0], vo[1], vo[2], sidx, wO, tt, stv_, rx, 0u);
      CST(2);
      v2u uf[3];
#pragma unroll
      for (int t = 0; t < 3; ++t) { uf[t] = (v2u){pk2(u[t][0], u[t][1]), pk2(u[t][2], u[t][3])}; asm volatile("" : "+v"(uf[t])); }
      if (cw < 2) *(LAS v2u*)(lds + CL_UY + cw * 2048 + 1024 + lane * 8) = uf[2];
      __builtin_amdgcn_sched_barrier(0);
      CST(3);
      __builtin_amdgcn_s_waitcnt(0xC07F); __builtin_amdgcn_s_barrier();
      asm volatile("" ::: "memory");
      CST(4);
      final_tiles(IC<2>{}, uf, yf, x2a);
      __builtin_amdgcn_sched_barrier(0);
      tdl = NOW() + 24 * a.d3;
      STAMPL(6);
      CST(5);
      POLL_CHECK(stv_);
      __builtin_amdgcn_s_setprio(0);
      computeY((s + 1) & 1);
      if (cw == 3) stx4(rx, x1addr(slp, po9), 0, SENT);
      __builtin_amdgcn_s_setprio(2);
      CST(6);
      STAMP_FLUSH(s);
      sl = sl == 2 ? 0 : sl + 1;
    }
  }
  asm volatile("s_waitcnt vmcnt(0)" ::: "memory");
  __builtin_amdgcn_s_barrier();
  if (tid == 0) {
    __hip_atomic_fetch_add(CNT(ctl, CTL_KM + (NSEG - 1) * NH + h), 1u, RLX_AGENT);
  }
}

extern "C" void kernel_launch(void* const* d_in, const int* in_sizes, int n_in, void* d_out, int out_size, void* d_ws, size_t ws_size,
                              hipStream_t stream) {
  const u16* x = (const u16*)d_in[0];
  const u16* mem0 = (const u16*)d_in[1];
  const u16* Wq = (const u16*)d_in[2];
  const u16* Wk = (const u16*)d_in[3];
  const u16* Wv = (const u16*)d_in[4];
  const u16* Wo = (const u16*)d_in[5];
  const u16* w1 = (const u16*)d_in[8];
  const u16* b1 = (const u16*)d_in[9];
  const u16* w2 = (const u16*)d_in[10];
  const u16* b2 = (const u16*)d_in[11];
  unsigned char* ws = (unsigned char*)d_ws;
  (void)hipFuncSetAttribute((const void*)mega_kernel, hipFuncAttributeMaxDynamicSharedMemorySize, CH_LDS);
  int npf = 0;
  prologue_kernel<<<625 + npf, 256, 0, stream>>>(x, ws, (const v4u*)Wk, (const v4u*)Wv, (const v4u*)Wq, (const v4u*)Wo);
  MegaArgs ca; ca.Wq = Wq; ca.Wk = Wk; ca.Wv = Wv; ca.Wo = Wo; ca.mem0 = mem0; ca.w1 = w1; ca.b1 = b1; ca.x = x; ca.b2 = b2; ca.w2 = w2; ca.out = (u16*)d_out; ca.ws = ws;
  ca.d1 = 25; ca.d1q = 25; ca.d1v = 32; ca.d2 = 46; ca.d3 = 95; ca.d4 = 10; ca.stag = 150; ca.pad = 0;
  mega_kernel<<<256, 512, CH_LDS, stream>>>(ca);

}
```
